```python
import torch

def _softmax(x, axis=-1):
    return torch.softmax(x, dim=axis)
def _gelu(x):
    return torch.nn.functional.gelu(x, approximate="tanh")
def _silu(x):
    return torch.nn.functional.silu(x)
def _sigmoid(x):
    return torch.sigmoid(x)
def _layernorm(x, axis=-1, eps=1e-5):
    m = x.mean(dim=axis, keepdim=True)
    v = x.var(dim=axis, keepdim=True, unbiased=False)
    return (x - m) / torch.sqrt(v + eps)
def _scatter_add(tgt, idx, src, axis=0):
    return tgt.clone().index_add_(axis, idx, src)
def _segment_sum(src, seg, n):
    out = torch.zeros((n,) + src.shape[1:], dtype=src.dtype)
    return out.index_add_(0, seg, src)
def _pad(x, pw):
    flat = tuple(v for pair in reversed(pw) for v in pair)
    return torch.nn.functional.pad(x, flat)

kernel_name = 'parallel_sum__sliding_block__dense_activation_gelu__prompt'

def setup_inputs():

    _w = (lambda a: (lambda t: t.to(torch.bfloat16) if t.is_floating_point() else t)(torch.as_tensor(a)))
    def _A_setup():
        B, S, D = 1, 2048, 512
        return {
            "x": _w((lambda *s: torch.randn(*s, dtype=torch.bfloat16) * 0.05)(B, S, D)),
            "wqkv": _w((lambda *s: torch.randn(*s, dtype=torch.bfloat16) * 0.05)(D, 3 * D)),
            "wo": _w((lambda *s: torch.randn(*s, dtype=torch.bfloat16) * 0.05)(D, D)),
        }
    _A_in = _A_setup()

    def _B_setup():
        B, S, D, F = 1, 2048, 512, 1792
        return {
            "x": _w((lambda *s: torch.randn(*s, dtype=torch.bfloat16) * 0.05)(B, S, D)),
            "w1": _w((lambda *s: torch.randn(*s, dtype=torch.bfloat16) * 0.8)(D, F) * (D ** -0.5)),
            "b1": _w((lambda *s: torch.randn(*s, dtype=torch.bfloat16) * 0.8)(F) * 0.01),
            "w2": _w((lambda *s: torch.randn(*s, dtype=torch.bfloat16) * 0.8)(F, D) * (F ** -0.5)),
            "b2": _w((lambda *s: torch.randn(*s, dtype=torch.bfloat16) * 0.8)(D) * 0.01),
        }
    _B_in = _B_setup()

    return {
        "A_x": _A_in["x"],
        "A_wqkv": _A_in["wqkv"],
        "A_wo": _A_in["wo"],
        "B_x": _B_in["x"],
        "B_w1": _B_in["w1"],
        "B_b1": _B_in["b1"],
        "B_w2": _B_in["w2"],
        "B_b2": _B_in["b2"],
    }

def reference(A_x, A_wqkv, A_wo, B_x, B_w1, B_b1, B_w2, B_b2):
    def _A_ref(x, wqkv, wo):
        B, S, D = x.shape
        H = 32; Dh = D // H; W = 256
        qkv = (lambda x, *d: x.permute(*d))((x @ wqkv).reshape(B, S, 3, H, Dh), 2, 0, 3, 1, 4)
        q, k, v = qkv[0], qkv[1], qkv[2]
        scores = torch.einsum("bhqd,bhkd->bhqk", q, k) * (Dh ** -0.5)
        idx = torch.arange(S)
        mask = (idx[:, None] - idx[None, :] >= 0) & (idx[:, None] - idx[None, :] < W)
        scores = torch.where(mask[None, None], scores, -1e9)
        p = _softmax(scores, axis=-1)
        o = torch.einsum("bhqk,bhkd->bhqd", p, v)
        return (lambda x, *d: x.permute(*d))(o, 0, 2, 1, 3).reshape(B, S, D) @ wo

    def _B_ref(x, w1, b1, w2, b2):
        h = torch.einsum("bsd,df->bsf", x, w1) + b1
        h = _gelu(h)
        return torch.einsum("bsf,fd->bsd", h, w2) + b2

    x = A_x
    xn = _layernorm(x, axis=-1)
    return x + _A_ref(xn, A_wqkv, A_wo) + _B_ref(xn, B_w1, B_b1, B_w2, B_b2)
```

```cpp
#include <hip/hip_runtime.h>
#include <hip/hip_bf16.h>
#include <stdint.h>
typedef unsigned short u16;
namespace naive {
__device__ __forceinline__ float bf2f(u16 v) { return __uint_as_float(((unsigned)v) << 16); }
__device__ __forceinline__ u16 f2bf(float f) { unsigned u = __float_as_uint(f); u += 0x7fffu + ((u >> 16) & 1u); return (u16)(u >> 16); }
__device__ __forceinline__ float rbf(float f) { return bf2f(f2bf(f)); }
constexpr int S = 2048, D = 512, F = 1792, H = 32, DH = 16, W = 256;

__global__ void ln_kernel(const u16* x, u16* xn) {
  int row = blockIdx.x; int t = threadIdx.x;
  float v[8]; float s = 0;
  for (int i = 0; i < 8; ++i) { v[i] = bf2f(x[row * D + t * 8 + i]); s += v[i]; }
  for (int o = 32; o; o >>= 1) s += __shfl_xor(s, o);
  float mean = s / D; float q = 0;
  for (int i = 0; i < 8; ++i) { float d = v[i] - mean; q += d * d; }
  for (int o = 32; o; o >>= 1) q += __shfl_xor(q, o);
  float var = rbf(q / D); float m = rbf(mean);
  float den = rbf(sqrtf(rbf(var + 1e-5f)));
  for (int i = 0; i < 8; ++i) xn[row * D + t * 8 + i] = f2bf(rbf(v[i] - m) / den);
}
__global__ void gemm_kernel(const u16* A, const u16* B, const u16* bias, u16* C, int N, int K, int mode) {
  int row = blockIdx.y; int col = blockIdx.x * 256 + threadIdx.x;
  if (col >= N) return;
  float acc = 0;
  for (int k = 0; k < K; ++k) acc += bf2f(A[row * K + k]) * bf2f(B[k * N + col]);
  float r = rbf(acc);
  if (mode >= 1) r = rbf(r + bf2f(bias[col]));
  if (mode == 1) { float u = 0.7978845608028654f * (r + 0.044715f * r * r * r); r = 0.5f * r * (1.f + tanhf(u)); }
  C[row * N + col] = f2bf(r);
}
__global__ void attn_kernel(const u16* qkv, u16* o) {
  int row = blockIdx.x; int h = threadIdx.x;
  float q[DH]; for (int d = 0; d < DH; ++d) q[d] = bf2f(qkv[row * 3 * D + h * DH + d]);
  int k0 = row - W + 1; if (k0 < 0) k0 = 0;
  float m = -1e30f;
  for (int k = k0; k <= row; ++k) { float s = 0; for (int d = 0; d < DH; ++d) s += q[d] * bf2f(qkv[k * 3 * D + D + h * DH + d]); s = rbf(s) * 0.25f; m = fmaxf(m, s); }
  float l = 0;
  for (int k = k0; k <= row; ++k) { float s = 0; for (int d = 0; d < DH; ++d) s += q[d] * bf2f(qkv[k * 3 * D + D + h * DH + d]); s = rbf(s) * 0.25f; l += expf(s - m); }
  float acc[DH]; for (int d = 0; d < DH; ++d) acc[d] = 0;
  for (int k = k0; k <= row; ++k) { float s = 0; for (int d = 0; d < DH; ++d) s += q[d] * bf2f(qkv[k * 3 * D + D + h * DH + d]); s = rbf(s) * 0.25f; float p = rbf(expf(s - m) / l);
    for (int d = 0; d < DH; ++d) acc[d] += p * bf2f(qkv[k * 3 * D + 2 * D + h * DH + d]); }
  for (int d = 0; d < DH; ++d) o[row * D + h * DH + d] = f2bf(acc[d]);
}
__global__ void final_kernel(const u16* x, const u16* a, const u16* b, u16* out) {
  int i = blockIdx.x * 256 + threadIdx.x;
  out[i] = f2bf(rbf(bf2f(x[i]) + bf2f(a[i])) + bf2f(b[i]));
}
inline void launch(void* const* d_in, void* d_out, void* d_ws, hipStream_t stream) {
  const u16* x = (const u16*)d_in[0]; const u16* wqkv = (const u16*)d_in[1]; const u16* wo = (const u16*)d_in[2];
  const u16* w1 = (const u16*)d_in[4]; const u16* b1 = (const u16*)d_in[5]; const u16* w2 = (const u16*)d_in[6]; const u16* b2 = (const u16*)d_in[7];
  u16* ws = (u16*)d_ws;
  u16* xn = ws; u16* qkv = xn + S * D; u16* o = qkv + S * 3 * D; u16* hh = o + S * D; u16* a = hh + S * F; u16* b = a + S * D;
  ln_kernel<<<S, 64, 0, stream>>>(x, xn);
  gemm_kernel<<<dim3(6, S), 256, 0, stream>>>(xn, wqkv, nullptr, qkv, 3 * D, D, 0);
  attn_kernel<<<S, 32, 0, stream>>>(qkv, o);
  gemm_kernel<<<dim3(2, S), 256, 0, stream>>>(o, wo, nullptr, a, D, D, 0);
  gemm_kernel<<<dim3(7, S), 256, 0, stream>>>(xn, w1, b1, hh, F, D, 1);
  gemm_kernel<<<dim3(2, S), 256, 0, stream>>>(hh, w2, b2, b, D, F, 2);
  final_kernel<<<S * D / 256, 256, 0, stream>>>(x, a, b, (u16*)d_out);
}
}
extern "C" void kernel_launch(void* const* d_in, const int* in_sizes, int n_in, void* d_out, int out_size, void* d_ws, size_t ws_size, hipStream_t stream) {
  naive::launch(d_in, d_out, d_ws, stream);
}
```

```cpp
#include <hip/hip_runtime.h>
#include <stdint.h>

#if defined(__HIP_DEVICE_COMPILE__)
#pragma clang attribute push(__attribute__((target("no-packed-fp32-ops"))), apply_to = function)
#endif
namespace fk {
typedef unsigned short u16;
typedef unsigned long long u64;
typedef short bf16x8 __attribute__((ext_vector_type(8)));
typedef short s16x4 __attribute__((ext_vector_type(4)));
typedef float f32x2 __attribute__((ext_vector_type(2)));
typedef float f32x4 __attribute__((ext_vector_type(4)));
typedef float f32x16 __attribute__((ext_vector_type(16)));
typedef unsigned u32x2 __attribute__((ext_vector_type(2)));
typedef unsigned u32x4 __attribute__((ext_vector_type(4)));
typedef __bf16 bf16x2_t __attribute__((ext_vector_type(2)));
#define LAS __attribute__((address_space(3)))
#define GAS __attribute__((address_space(1)))
#define DI __device__ __forceinline__
typedef LAS char* lptr;

constexpr int S = 2048, D = 512, F = 1792;
constexpr unsigned WS_EPOCH = 0;
constexpr unsigned WS_FQKV = 256;
constexpr unsigned WS_FH = WS_FQKV + 16 * 256;
constexpr unsigned WS_FO = WS_FH + 16 * 128;
constexpr unsigned WS_DBG = 16384;
constexpr unsigned WS_QKV = 1u << 20;
constexpr unsigned WS_HO = WS_QKV + 6u * (1u << 20);
constexpr unsigned WS_END = WS_HO + 32u * 72u * 4096u;
constexpr int SLOT = 16384, NSLOT = 9, MISC = NSLOT * SLOT;
constexpr int M_STATS = MISC, M_B1 = MISC + 1024  , M_B2 = MISC + 1536, M_EP = MISC + 1792, M_ONES = MISC + 2048  , M_CNT = MISC + 3072,
              M_JUNK = MISC + 3584, M_CS = MISC + 4096  , M_Q = MISC + 8192  ;
constexpr int LDS_BYTES = 163840;

#define MFMA16(a, b, c) __builtin_amdgcn_mfma_f32_16x16x32_bf16((a), (b), (c), 0, 0, 0)
#define MFMA16K16(a, b, c) __builtin_amdgcn_mfma_f32_16x16x16bf16_1k((a), (b), (c), 0, 0, 0)
#define MFMA32(a, b, c) __builtin_amdgcn_mfma_f32_32x32x16_bf16((a), (b), (c), 0, 0, 0)
#define SBAR() __builtin_amdgcn_sched_barrier(0)
#define BAR() asm volatile("s_waitcnt lgkmcnt(0)\n\ts_barrier" ::: "memory")
#define VMW(N) asm volatile("s_waitcnt vmcnt(" #N ")" ::: "memory")
#define RLX_AGENT __ATOMIC_RELAXED, __HIP_MEMORY_SCOPE_AGENT

typedef __amdgpu_buffer_rsrc_t rsrc_t;
DI unsigned rfl(unsigned v) { return __builtin_amdgcn_readfirstlane(v); }
DI rsrc_t mkrsrc(const void* p, unsigned bytes) { return __builtin_amdgcn_make_buffer_rsrc((void*)p, 0, bytes, 0x00020000); }
DI void setm0(unsigned lds_dst) { asm volatile("s_mov_b32 m0, %0\n\ts_nop 0" ::"s"(rfl(lds_dst)) : "memory"); }
template <int OFF> DI void pc16(rsrc_t rs, unsigned voff, unsigned soff) {
  asm volatile("buffer_load_dwordx4 %0, %1, %2 offen offset:%3 lds" ::"v"(voff), "s"(rs), "s"(rfl(soff)), "i"(OFF) : "memory"); }
template <int OFF> DI void pc16s(rsrc_t rs, unsigned voff, unsigned soff) {
  asm volatile("buffer_load_dwordx4 %0, %1, %2 offen offset:%3 sc1 lds" ::"v"(voff), "s"(rs), "s"(rfl(soff)), "i"(OFF) : "memory"); }
DI void item16(rsrc_t rs, const unsigned (&v)[4], unsigned soff, unsigned dst) { setm0(dst); pc16<0>(rs, v[0], soff); pc16<1024>(rs, v[1], soff); pc16<2048>(rs, v[2], soff); pc16<3072>(rs, v[3], soff); }
DI void item16s(rsrc_t rs, unsigned v, unsigned soff, unsigned dst) { setm0(dst); pc16s<0>(rs, v, soff); pc16s<1024>(rs, v, soff); pc16s<2048>(rs, v, soff); pc16s<3072>(rs, v, soff); }
DI void dma4(rsrc_t rs, unsigned voff, unsigned lds_dst) { setm0(lds_dst); asm volatile("buffer_load_dword %0, %1, 0 offen lds" ::"v"(voff), "s"(rs) : "memory"); }
DI void dma4_sc1(rsrc_t rs, unsigned voff, unsigned lds_dst) { setm0(lds_dst); asm volatile("buffer_load_dword %0, %1, 0 offen sc1 lds" ::"v"(voff), "s"(rs) : "memory"); }
DI void st16_sc1(u32x4 v, rsrc_t rs, unsigned voff, unsigned soff) {
  asm volatile("buffer_store_dwordx4 %0, %1, %2, %3 offen sc1\n\ts_nop 1" ::"v"(v), "v"(voff), "s"(rs), "s"(rfl(soff)) : "memory"); }
DI void set_flag(u32x2 tag, rsrc_t rs, unsigned soff) {
  asm volatile("s_mov_b64 exec, 1\n\tbuffer_store_dwordx2 %0, off, %1, %2 sc1\n\ts_mov_b64 exec, -1" ::"v"(tag), "s"(rs), "s"(rfl(soff)) : "memory"); }
DI void count_flag(unsigned cnt_addr, unsigned last, u32x2 tag, rsrc_t rs, unsigned soff) {
  unsigned t;
  asm volatile("s_mov_b64 exec, 1\n\tv_mov_b32 %0, 1\n\tds_add_rtn_u32 %0, %1, %0\n\ts_waitcnt lgkmcnt(0)\n\tv_cmp_eq_u32 vcc, %2, %0\n\ts_cbranch_vccz 1f\n\t"
               "buffer_store_dwordx2 %3, off, %4, %5 sc1\n1:\n\ts_mov_b64 exec, -1"
               : "=&v"(t) : "v"(cnt_addr), "i"(last), "v"(tag), "s"(rs), "s"(rfl(soff)) : "vcc", "memory"); }

DI bf16x8 ld128(lptr p) { return *(LAS bf16x8*)p; }
DI s16x4 ldtr(lptr p) { return __builtin_amdgcn_ds_read_tr16_b64_v4i16((LAS s16x4*)p); }
DI bf16x8 cat8(s16x4 a, s16x4 b) { return __builtin_shufflevector(a, b, 0, 1, 2, 3, 4, 5, 6, 7); }
DI bf16x8 ldtr2(lptr p, int second) { return cat8(ldtr(p), ldtr(p + second)); }
DI unsigned pk(float lo, float hi) { f32x2 f = {lo, hi}; return __builtin_bit_cast(unsigned, __builtin_convertvector(f, bf16x2_t)); }
DI float u2f(unsigned u) { return __builtin_bit_cast(float, u); }
DI unsigned f2u(float f) { return __builtin_bit_cast(unsigned, f); }
DI float bflo(unsigned u) { return u2f(u << 16); }
DI float bfhi(unsigned u) { return u2f(u & 0xffff0000u); }
DI unsigned split2(float v) { const unsigned h = pk(v, 0.f); return pk(v, v - bflo(h)); }
DI lptr lxor(lptr p, unsigned m) { return (lptr)(uintptr_t)((unsigned)(uintptr_t)p ^ m); }
DI bool all_lanes(bool c) { return __builtin_amdgcn_ballot_w64(!c) == 0; }

DI void poll(const char* ws, unsigned off, unsigned want) {
  GAS unsigned* p = (GAS unsigned*)(ws + off);
#pragma clang loop unroll(disable)
  for (unsigned spins = 0; spins < (1u << 16); ++spins) {
    if (all_lanes(__hip_atomic_load(p, RLX_AGENT) == want)) break;
    __builtin_amdgcn_s_sleep(2);
  }
  asm volatile("" ::: "memory");
}

#define FT(i) do { } while (0)
#define STAMP(i) do { } while (0)

__global__ __launch_bounds__(512, 2) void fused(const char* __restrict__ x, const char* __restrict__ wqkv, const char* __restrict__ wo,
                                                const char* __restrict__ w1, const char* __restrict__ b1, const char* __restrict__ w2,
                                                const char* __restrict__ b2, char* __restrict__ out, char* ws) {
  __shared__ __attribute__((aligned(1024))) char smem_[LDS_BYTES];
  const lptr smem = (lptr)smem_;
  const unsigned lds0 = (unsigned)(uintptr_t)smem;
  const int tid = __builtin_amdgcn_workitem_id_x(), l = tid & 63;
  const int w = __builtin_amdgcn_readfirstlane(tid >> 6);
  const int b = __builtin_amdgcn_workgroup_id_x(), xcd = b & 7, jj = b >> 3;
  const int r = (xcd & 1) * 8 + (jj & 7), type = jj >> 3, uq = xcd >> 1;
  const int hp = uq * 4 + type;
  const int rh = xcd * 4 + (jj & 3), ct = jj >> 2;
  const bool q1 = type < 2;
  const int kind0 = 1 + (type >> 1), hq = 2 * uq + (type & 1);
  const int f2 = uq * 7 + (q1 ? type : 2 * type - 1), f3 = uq * 7 + 6;
  const int rg = w & 3, ch = w >> 2, m0 = type & 1;
  STAMP(0); FT(0);
  const unsigned colsw = (unsigned)(((((l & 7) >> 1) ^ ((l >> 4) & 3)) << 5) + ((l & 1) << 4));
  const unsigned rowW = (unsigned)(8 * (l >> 5) + ((l >> 3) & 3) + 32 * rg);
  const rsrc_t r1 = mkrsrc(w1, D * F * 2), wsr = mkrsrc(ws, WS_END);
  const unsigned ldw = lds0 + rg * 4096;
  const int t16 = l & 15, g4 = l >> 4;
  const int aB0 = (4 * g4 + ((l >> 2) & 3)) * 128 + 8 * (l & 3) + ((((g4 & 1) << 1) | ((l >> 3) & 1)) << 5);
  const int aX0 = (32 * rg + 16 * m0 + t16) * 128 + ((g4 ^ (t16 >> 1)) << 4);
  bf16x8 ones = {0x3f80, 0x3f80, 0x3f80, 0x3f80, 0x3f80, 0x3f80, 0x3f80, 0x3f80};
  bf16x8 xf[2][16];
  f32x4 acc[2][4], cs[4];
  s16x4 h[2][4][2], cor[2];
  float rstd_[2], std_[2];
  u32x2 tag;
  unsigned vF[4];
  int ac[4];
  auto setup = [&]() {
#pragma unroll
    for (int c = 0; c < 4; ++c) { vF[c] = (rowW + 16 * (c & 1) + 4 * (c >> 1)) * 3584u + colsw - 1024u * c; ac[c] = (aB0 ^ (32 * c)) + rg * 4096;
      asm volatile("" : "+v"(vF[c]), "+v"(ac[c])); }
    asm volatile("" : "+v"(ones)); };
  auto cs_item = [&](int off) {
#pragma unroll
    for (int n = 0; n < 4; ++n) cs[n] = MFMA16(ones, ldtr2(smem + ac[n] + off, 2048), cs[n]); };
  auto cs_move = [&](int d) {
#pragma unroll
    for (int n = 0; n < 4; ++n) { ac[n] += d; asm volatile("" : "+v"(ac[n])); } };
  auto put_cs = [&](int buf, unsigned bv) {
    const float c0 = cs[0][0], c1 = cs[1][0], c2 = cs[2][0], c3 = cs[3][0];
    const float lo = (g4 & 1) ? c1 : c0, hi = (g4 & 1) ? c3 : c2;
    const unsigned hl = split2((g4 & 2) ? hi : lo);
    *(LAS u32x2*)(smem + M_CS + buf * 2048 + rg * 512 + l * 8) = u32x2{hl, (hl & 0xffffu) | (bv << 16)}; };
  auto bias16 = [&](int boff, bool on) -> unsigned { const unsigned v = *(LAS u16*)(smem + M_B1 + boff + 2 * l); return on ? v : 0u; };
  auto fix_acc = [&]() {
    s16x4 a[4];
#pragma unroll
    for (int n = 0; n < 4; ++n) a[n] = *(LAS s16x4*)(smem + M_CS + ch * 2048 + g4 * 512 + (16 * n + t16) * 8);
#pragma unroll
    for (int n = 0; n < 4; ++n) { acc[0][n] = MFMA16K16(a[n], cor[0], acc[0][n]); acc[1][n] = MFMA16K16(a[n], cor[1], acc[1][n]); } };
  auto flags_get = [&](unsigned off) { dma4_sc1(wsr, off, lds0 + M_Q + w * 1024); };
  auto flags_chk = [&](unsigned off) { const unsigned want = (l & 1) ? tag[1] : tag[0];
    if (__builtin_expect(!all_lanes(*(LAS unsigned*)(smem + M_Q + w * 1024 + l * 4) == want), 0)) poll(ws, off, want); };
  auto qkv_head = [&](int n, float rs0, float rs1, unsigned lo, unsigned so) {
    const f32x4 y0 = acc[0][n] * rs0, y1 = acc[1][n] * rs1;
    auto s0 = __builtin_amdgcn_permlane16_swap(pk(y0[0], y0[1]), pk(y1[0], y1[1]), false, false);
    auto s1 = __builtin_amdgcn_permlane16_swap(pk(y0[2], y0[3]), pk(y1[2], y1[3]), false, false);
    st16_sc1(u32x4{s0[0], s1[0], s0[1], s1[1]}, wsr, lo, so + n * 65536u); };
  auto gelu8 = [&](f32x4 a, f32x4 c, int m) -> u32x4 {
    const float c1 = -2.f * 0.7978845608028654f * 1.4426950408889634f, k1 = c1 * rstd_[m], k2 = (c1 * 0.044715f) * rstd_[m] * rstd_[m] * rstd_[m], sd = std_[m];
    float y[8];
#pragma unroll
    for (int e = 0; e < 8; ++e) { const float v = e < 4 ? a[e & 3] : c[e & 3];
      const float ex = __builtin_amdgcn_exp2f(v * __builtin_fmaf(v * v, k2, k1));
      y[e] = v * __builtin_amdgcn_rcpf(__builtin_fmaf(ex, sd, sd)); }
    return u32x4{pk(y[0], y[1]), pk(y[2], y[3]), pk(y[4], y[5]), pk(y[6], y[7])}; };
  auto get_stats = [&]() {
    const u32x2 ep = *(LAS u32x2*)(smem + M_EP);
    const u64 t = (((u64)ep[1] << 32) | ep[0]) + 1; tag = u32x2{(unsigned)t, (unsigned)(t >> 32)};
#pragma unroll
    for (int m = 0; m < 2; ++m) { const f32x2 s2 = *(LAS f32x2*)(smem + M_STATS + (32 * rg + 16 * m + t16) * 8);
      const float mean = s2[0] * (1.f / D), ve = __builtin_fmaxf(__builtin_fmaf(s2[1], 1.f / D, -mean * mean), 0.f) + 1e-5f;
      rstd_[m] = __builtin_amdgcn_rsqf(ve); std_[m] = ve * rstd_[m];
      const unsigned mm = split2(-mean), ss = split2(std_[m]);
      cor[m] = __builtin_bit_cast(s16x4, u32x2{(mm & 0xffffu) | (mm << 16), (mm >> 16) | (g4 == 0 ? ss << 16 : ss & 0xffff0000u)}); } };
  const unsigned sF2 = (unsigned)f2 * 128u, sF3 = (unsigned)f3 * 128u;
  constexpr unsigned QF = 128 * 3584;

  if (ch) {
    unsigned vQ[4], vX[4];
#pragma unroll
    for (int c = 0; c < 4; ++c) { vX[c] = (unsigned)((32 * rg + (l >> 3)) * 1024 + c * 7168 + (((l & 7) ^ ((4 * (c & 1) + (l >> 4)) & 7)) << 4));
      vQ[c] = (rowW + 16 * (c & 1) + 4 * (c >> 1)) * 3072u + colsw - 1024u * c; }
    const rsrc_t rx = mkrsrc(x, S * D * 2), rq = mkrsrc(wqkv, D * 3 * D * 2);
    const unsigned c0 = (unsigned)(kind0 * 1024 + hq * 128), xo = (unsigned)r * 131072u;
    auto trip = [&](int u, int i) { const int s0 = (3 * u) % NSLOT;
      if (i < 2) item16(rx, vX, xo + u * 256 + i * 128, ldw + (s0 + i) * SLOT); else item16(rq, vQ, c0 + u * (128 * 3072), ldw + (s0 + 2) * SLOT); };
    trip(0, 0); trip(0, 2); trip(0, 1); trip(1, 0); trip(1, 2); trip(1, 1);
    setup();
    const rsrc_t rs1 = mkrsrc(q1 ? wqkv : w1, q1 ? D * 3 * D * 2 : D * F * 2);
    const unsigned c1 = (unsigned)(q1 ? hq : f2 - 1) * 128u, st1 = q1 ? 128 * 3072 : QF;
    f32x4 sm[2] = {}, sq[2] = {};
    cs[0] = cs[1] = cs[2] = cs[3] = f32x4{};
#pragma unroll
    for (int u = 0; u < 4; ++u) {
      const int s0 = (3 * u) % NSLOT;
      if (u == 2) {
#pragma unroll
        for (int c = 0; c < 4; ++c) vQ[c] = q1 ? vQ[c] : vF[c]; }
      FT(1 + 3 * u); VMW(12); BAR(); FT(3 + 3 * u);
#pragma unroll
      for (int c = 0; c < 4; ++c) {
        const int k = 4 * u + c;
        const lptr xp = smem + (s0 + (c >> 1)) * SLOT + (aX0 ^ (64 * (c & 1)));
        xf[0][k] = ld128(xp); xf[1][k] = ld128(lxor(xp, 2048));
        const bf16x8 cf = ldtr2(smem + (s0 + 2) * SLOT + ac[c], 2048);
        SBAR();
        if (c < 3) {
          if (u < 2) trip(u + 2, c);
          if (u == 2) item16(rs1, vQ, c1 + c * st1, ldw + (3 + c) * SLOT);
          if (u == 3) { if (c == 0) item16(rs1, vQ, c1 + 3 * st1, ldw + 6 * SLOT); else item16(r1, vF, sF2 + (c - 1) * QF, ldw + (6 + c) * SLOT); }
        }
        sm[0] = MFMA16(ones, xf[0][k], sm[0]); sq[0] = MFMA16(xf[0][k], xf[0][k], sq[0]);
        sm[1] = MFMA16(ones, xf[1][k], sm[1]); sq[1] = MFMA16(xf[1][k], xf[1][k], sq[1]);
        cs[c] = MFMA16(ones, cf, cs[c]);
        SBAR();
      }
    }
    { const bool e0 = l & 1, e1 = l & 2;
      const float d0 = e1 ? (e0 ? sq[0][3] : sq[0][2]) : (e0 ? sq[0][1] : sq[0][0]), d1 = e1 ? (e0 ? sq[1][3] : sq[1][2]) : (e0 ? sq[1][1] : sq[1][0]);
      const lptr sp = smem + (g4 == (t16 >> 2) ? M_STATS + (32 * rg + t16) * 8 : M_Q + w * 1024);
      *(LAS f32x2*)sp = f32x2{sm[0][0], d0}; *(LAS f32x2*)(sp + 128) = f32x2{sm[1][0], d1}; }
    put_cs(0, 0);
    FT(13); VMW(12);
  } else {
    if (w == 0) dma4_sc1(wsr, (unsigned)(l & 1) * 4u, lds0 + M_EP);
    if (w == 1) { const rsrc_t rb = mkrsrc(b1, F * 2); dma4(rb, (unsigned)(l * 4 + (f2 - 1) * 128), lds0 + M_B1); dma4(rb, (unsigned)(l * 4 + f3 * 128), lds0 + M_B1 + 256); }
    if (w == 2) dma4(mkrsrc(b2, D * 2), (unsigned)(l * 4 + ct * 128), lds0 + M_B2);
    if (w == 3) { *(LAS u32x4*)(smem + M_ONES + l * 16) = u32x4{0x3f803f80u, 0x3f803f80u, 0x3f803f80u, 0x3f803f80u}; *(LAS unsigned*)(smem + M_CNT + l * 4) = 0u; }
    setup();
#pragma unroll
    for (int m = 0; m < 2; ++m)
#pragma unroll
      for (int n = 0; n < 4; ++n) acc[m][n] = f32x4{};
    FT(2); BAR(); FT(3);
    xf[0][0] = ld128(smem + aX0); xf[1][0] = ld128(smem + (aX0 ^ 2048));
#pragma unroll
    for (int n = 0; n < 4; ++n) { h[0][n][0] = ldtr(smem + 2 * SLOT + (aB0 ^ (32 * n))); h[0][n][1] = ldtr(smem + 2 * SLOT + (aB0 ^ (32 * n)) + 2048); }
#pragma unroll
    for (int k = 0; k < 16; ++k) {
      const int kn = k + 1, sn = (3 * (kn >> 2)) % NSLOT, c = k & 1;
      if ((k & 3) == 3 && k < 15) { FT(2 + 3 * (kn >> 2)); BAR(); FT(3 + 3 * (kn >> 2)); }
      const lptr xp = smem + (sn + ((kn & 3) >> 1)) * SLOT, wb = smem + (sn + 2) * SLOT + (kn & 3) * 4096;
#pragma unroll
      for (int n = 0; n < 4; ++n) {
        const bf16x8 bf = cat8(h[c][n][0], h[c][n][1]);
        if (k < 15) { h[c ^ 1][n][0] = ldtr(wb + (aB0 ^ (32 * n))); if (n < 2) xf[n][kn] = ld128(xp + (aX0 ^ (64 * (kn & 1)) ^ (2048 * n))); }
        SBAR(); acc[0][n] = MFMA16(bf, xf[0][k], acc[0][n]); SBAR();
        if (k < 15) h[c ^ 1][n][1] = ldtr(wb + (aB0 ^ (32 * n)) + 2048);
        SBAR(); acc[1][n] = MFMA16(bf, xf[1][k], acc[1][n]); SBAR();
      }
    }
    VMW(0);
  }
  const int tok = (l & 31) ^ (16 * m0);
  const unsigned qso = WS_QKV + (unsigned)(hq * 256 + 4 * r + rg) * 1024u;
  FT(14); BAR(); FT(15);
  STAMP(1);

  if (ch == 0) {
    item16(r1, vF, sF2 + 2 * QF, ldw);
    get_stats();
    cs[0] = cs[1] = cs[2] = cs[3] = f32x4{};
    cs_move(3 * SLOT);
    FT(16); fix_acc(); SBAR();
    item16(r1, vF, sF2 + 3 * QF, ldw + SLOT);
    cs_item(0); SBAR();
    const unsigned lo = kind0 == 2 ? tok * 32 + (l >> 5) * 16 : (l >> 5) * 512 + tok * 16, so = qso + kind0 * (32u * 65536u);
    qkv_head(0, rstd_[0], rstd_[1], lo, so); qkv_head(1, rstd_[0], rstd_[1], lo, so); SBAR();
    cs_item(SLOT); SBAR();
    FT(20); BAR(); FT(21);
    item16(r1, vF, sF3, ldw + 2 * SLOT);
    qkv_head(2, rstd_[0], rstd_[1], lo, so); qkv_head(3, rstd_[0], rstd_[1], lo, so); SBAR();
    item16(r1, vF, sF3 + QF, ldw + 3 * SLOT);
    const unsigned bv = bias16(0, !q1 && rg < 2);
    cs_item(2 * SLOT); SBAR();
    item16(r1, vF, sF3 + 2 * QF, ldw + 4 * SLOT);
    cs_item(3 * SLOT);
    put_cs(1, bv);
    VMW(8);
    FT(24); BAR(); FT(25);
  }
  {
    const int sa = ch ? 3 : 7, sb = ch ? 5 : 0;
    int a4[4];
#pragma unroll
    for (int n = 0; n < 4; ++n) { a4[n] = (aB0 ^ (32 * n)) + sa * SLOT; h[0][n][0] = ldtr(smem + a4[n]); h[0][n][1] = ldtr(smem + a4[n] + 2048); acc[0][n] = acc[1][n] = f32x4{}; }
#pragma unroll
    for (int k = 0; k < 16; ++k) {
      const int kn = k + 1, c = k & 1;
      if (k == 7) { FT(26); VMW(0); BAR(); FT(27);
#pragma unroll
        for (int n = 0; n < 4; ++n) a4[n] += (sb - sa) * SLOT; }
#pragma unroll
      for (int n = 0; n < 4; ++n) {
        const bf16x8 bf = cat8(h[c][n][0], h[c][n][1]);
        if (k < 15) h[c ^ 1][n][0] = ldtr(smem + a4[n] + (kn & 7) * 4096);
        SBAR(); acc[0][n] = MFMA16(bf, xf[0][k], acc[0][n]); SBAR();
        if (k < 15) h[c ^ 1][n][1] = ldtr(smem + a4[n] + (kn & 7) * 4096 + 2048);
        SBAR(); acc[1][n] = MFMA16(bf, xf[1][k], acc[1][n]); SBAR();
      }
    }
  }
  if (ch) get_stats();
  const unsigned voffL = (unsigned)l * 16u;
  const rsrc_t rw2 = mkrsrc(w2, F * D * 2);
  const unsigned qsrc = WS_QKV + 2 * hp * 65536;
  const unsigned fq = WS_FQKV + r * 256 + (hp >> 1) * 24 + (l & 1) * 4;
  const unsigned hbase = WS_HO + (unsigned)((2 * r + (rg >> 1)) * 288 + 2 * (rg & 1) + m0) * 1024u;
  unsigned vD[4];
#pragma unroll
  for (int c = 0; c < 4; ++c) vD[c] = (unsigned)((32 * rg + (l >> 3)) * 1024 + c * 7168 + ct * 128) + colsw;
  STAMP(2);
  FT(28); BAR(); FT(29);
  {
    const bool isq = ch && q1;
    const unsigned hs = hbase + (unsigned)(f2 - ch) * 8192u;
    const unsigned lo = (l >> 5) * 512 + tok * 16;
    const float sc = 0.25f * 1.4426950408889634f;
    int fr = r - 2 + ((l >> 2) & 3); fr = fr < 0 ? 0 : fr > r ? r : fr;
    const unsigned fkv = WS_FQKV + fr * 256 + (hp >> 1) * 24 + 8 + (l & 3) * 4;
    if (ch) { if (w == 4) set_flag(tag, wsr, WS_FQKV + r * 256 + (hq * 3 + kind0) * 8);
      item16(r1, vF, sF3 + 3 * QF, ldw + 5 * SLOT); cs_move(7 * SLOT); cs[0] = cs[1] = cs[2] = cs[3] = f32x4{}; }
    else flags_get(fq);
    fix_acc(); SBAR();
    FT(30);
    if (isq) { const float rs0 = rstd_[0] * sc, rs1 = rstd_[1] * sc;
#pragma unroll
      for (int n = 0; n < 4; ++n) qkv_head(n, rs0, rs1, lo, qso); }
    else { st16_sc1(gelu8(acc[0][0], acc[0][1], 0), wsr, voffL, hs); SBAR(); st16_sc1(gelu8(acc[1][0], acc[1][1], 1), wsr, voffL, hs ^ 1024u); }
    SBAR(); FT(31);
    if (ch) {
      cs_item(0); cs_item(SLOT); cs_move(-7 * SLOT);
      flags_get(fkv);
      if (q1) VMW(1);
      FT(36); BAR(); FT(37);
      if (q1 && w == 4) set_flag(tag, wsr, WS_FQKV + r * 256 + hq * 24);
      cs_item(0); SBAR();
      VMW(1); if (w != 4 || !q1) VMW(0);
      flags_chk(fkv);
#pragma unroll
      for (int j = 0; j < 3; ++j) { int rr = r - 2 + j; rr = rr < 0 ? 0 : rr;
        item16s(wsr, voffL, qsrc + ((rg & 1) + 32 + 32 * (rg >> 1)) * 65536 + rr * 4096, lds0 + 6 * SLOT + rg * 12288 + j * 4096);
        if (j == 0) cs_item(SLOT);
        if (j == 1) put_cs(0, bias16(128, rg < 2));
        SBAR(); }
    } else { VMW(2); flags_chk(fq); setm0(lds0 + M_Q + w * 1024); pc16s<0>(wsr, voffL, qsrc + (4 * r + rg) * 1024); }
    FT(39);
    if (!isq) { st16_sc1(gelu8(acc[0][2], acc[0][3], 0), wsr, voffL, hs + 4096); SBAR(); st16_sc1(gelu8(acc[1][2], acc[1][3], 1), wsr, voffL, (hs + 4096) ^ 1024u); }
    if (ch) { FT(44); BAR(); FT(45); }
  }
  if (ch) {
    flags_get(fq);
    FT(46);
    const lptr pa = smem + 2 * SLOT + (aB0 ^ (64 * (type >> 1))), pb = lxor(pa, 32);
    const lptr bp = smem + M_B1 + 256 + 64 * (type >> 1) + 2 * t16;
    unsigned ba = *(LAS u16*)bp, bb = *(LAS u16*)(bp + 32);
    f32x4 qa = {}, qb = {}, ca = {}, cb = {};
    bf16x8 fa = ldtr2(pa, 2048), fb = ldtr2(pb, 2048);
#pragma unroll
    for (int k = 0; k < 16; ++k) {
      const bf16x8 ga = fa, gb_ = fb;
      if (k < 15) { fa = ldtr2(pa + (k + 1) * 4096, 2048); fb = ldtr2(pb + (k + 1) * 4096, 2048); }
      if (k == 1 || k == 5) item16(rw2, vD, (k >> 2) * 131072, ldw + (k >> 2) * SLOT);
      if (k == 10) { VMW(8); flags_chk(fq); setm0(lds0 + M_Q + w * 1024); pc16s<0>(wsr, voffL, qsrc + 65536 + (4 * r + rg) * 1024); }
      SBAR();
      qa = MFMA16(ga, xf[0][k], qa); qb = MFMA16(gb_, xf[0][k], qb); ca = MFMA16(ones, ga, ca); cb = MFMA16(ones, gb_, cb);
      SBAR();
    }
    FT(47);
    ba = l < 32 ? ba << 16 : 0; bb = l < 32 ? bb << 16 : 0;
    const unsigned ha = split2(l < 16 ? ca[0] : 0.f), hb = split2(l < 16 ? cb[0] : 0.f);
    qa = MFMA16K16(__builtin_bit_cast(s16x4, u32x2{ha, (ha & 0xffffu) | ba}), cor[0], qa);
    qb = MFMA16K16(__builtin_bit_cast(s16x4, u32x2{hb, (hb & 0xffffu) | bb}), cor[0], qb);
    st16_sc1(gelu8(qa, qb, 0), wsr, voffL, hbase + (unsigned)f3 * 8192u + (type >> 1) * 4096);
    FT(48); VMW(1);
  } else VMW(2);
  const unsigned fho = (unsigned)((rh >> 1) * 128 + (l & 31) * 4);
  {
    const int e = ch, i = rg, hh = l >> 5, iq = l & 31;
    const lptr kp = smem + 6 * SLOT + e * 12288 + i * 1024 + l * 16;
    int bstart = 8 - 4 * r - i; bstart = bstart < 0 ? 0 : bstart;
    unsigned mk[8];
    { const int z = iq - 4 * hh;
#pragma unroll
      for (int c = 0; c < 8; ++c) { const int j0 = 2 * (c & 1) + 8 * (c >> 1); mk[c] = j0 + 1 <= z ? 0xffffffffu : j0 <= z ? 0xffffu : 0u; } }
    const bool onesl = (l & 16) != 0;
    const lptr vp8 = onesl ? smem + M_ONES + 8 * t16 : smem + 6 * SLOT + 24576 + e * 12288 + (i + 8) * 1024 + (4 * hh + (t16 >> 2)) * 32 + (l & 3) * 8;
    const int vstep = onesl ? 0 : 1024;
    const unsigned ovo = (unsigned)((iq >> 4) * 1024 + ((iq & 15) + 16 * (2 * e + hh)) * 16);
    STAMP(3);
    FT(49); BAR(); FT(50);
    STAMP(5);
    const bf16x8 qf = ld128(smem + M_Q + w * 1024 + l * 16), k8 = ld128(kp + 8 * 1024);
    const f32x16 zero = {};
    float m;
    { const f32x16 s = MFMA32(k8, qf, zero); m = u2f(__builtin_amdgcn_permlane32_swap(f2u(s[0]), f2u(s[0]), false, false)[0]); }
    f32x16 o;
#pragma clang loop unroll(disable)
    for (int attempt = 0;; ++attempt) {
      f32x16 nm;
#pragma unroll
      for (int c = 0; c < 16; ++c) nm[c] = -m;
      lptr vp = vp8;
      f32x16 sc = MFMA32(k8, qf, nm);
      bf16x8 kn = ld128(kp + 7 * 1024);
#pragma unroll
      for (int bb = 8; bb >= 0; --bb) {
        const bf16x8 v0 = ldtr2(vp, 256), v1 = ldtr2(vp + 512, 256);
        f32x16 sn;
        if (bb > 0) sn = MFMA32(kn, qf, nm);
        if (bb > 1) kn = ld128(kp + (bb - 2) * 1024);
        vp -= vstep;
        if (attempt == 0) {
          if (bb == 8) item16(rw2, vD, (2 + 2 * ch) * 131072, ldw + (2 + 2 * ch) * SLOT);
        }
        SBAR();
        unsigned p[8];
#pragma unroll
        for (int c = 0; c < 8; ++c) { p[c] = pk(__builtin_amdgcn_exp2f(sc[2 * c]), __builtin_amdgcn_exp2f(sc[2 * c + 1]));
          if (bb == 8) p[c] &= mk[c];
          if (bb == 0) p[c] &= ~mk[c];
          if (c == 3) o = MFMA32(v0, __builtin_bit_cast(bf16x8, u32x4{p[0], p[1], p[2], p[3]}), bb == 8 ? zero : o); }
        o = MFMA32(v1, __builtin_bit_cast(bf16x8, u32x4{p[4], p[5], p[6], p[7]}), o);
        SBAR();
        if (bb - 1 < bstart) break;
        if (bb == 5 && attempt == 0) { VMW(4); count_flag(lds0 + M_CNT, 7, tag, wsr, WS_FH + r * 128 + hp * 8); item16(rw2, vD, (3 + 2 * ch) * 131072, ldw + (3 + 2 * ch) * SLOT); }
        if (bb == 2 && ch) flags_get(WS_FH + fho);
        sc = sn;
      }
      if (attempt == 0 && bstart > 1) {
        if (bstart > 4) { VMW(4); count_flag(lds0 + M_CNT, 7, tag, wsr, WS_FH + r * 128 + hp * 8); item16(rw2, vD, (3 + 2 * ch) * 131072, ldw + (3 + 2 * ch) * SLOT); }
        if (ch) flags_get(WS_FH + fho); }
      if (__builtin_expect(attempt || all_lanes(o[8] < 1.0995116e12f), 1)) break;
#pragma clang loop unroll(disable)
      for (int bb = bstart; bb <= 8; ++bb) { const f32x16 s = MFMA32(ld128(kp + bb * 1024), qf, zero);
        const unsigned fl = bb == 0 ? 0xffffffffu : 0u, al = (bb == 0 || bb == 8) ? 0u : 0xffffffffu;
#pragma unroll
        for (int c = 0; c < 16; ++c) { const unsigned keep = (unsigned)((int)(((mk[c >> 1] ^ fl) | al) << (16 * (1 - (c & 1)))) >> 31);
          m = __builtin_fmaxf(m, u2f((f2u(s[c]) & keep) | (f2u(m) & ~keep))); } }
      { auto rr = __builtin_amdgcn_permlane32_swap(f2u(m), f2u(m), false, false); m = __builtin_fmaxf(u2f(rr[0]), u2f(rr[1])); }
    }
    STAMP(6); FT(51);
    const float rl = __builtin_amdgcn_rcpf(o[8]);
    const u32x4 ov = {pk(o[0] * rl, o[1] * rl), pk(o[2] * rl, o[3] * rl), pk(o[4] * rl, o[5] * rl), pk(o[6] * rl, o[7] * rl)};
    st16_sc1(ov, wsr, ovo, WS_HO + (unsigned)(((2 * r + (i >> 1)) * 72 + 56 + hp) * 4 + 2 * (i & 1)) * 1024u);
    if (ch) { VMW(1); flags_chk(WS_FH + fho); }
  }
  {
    f32x4 dacc[2][2] = {};
    const int mh = w & 1, nh = (w >> 1) & 1;
    const int row = tid >> 3, chunk = tid & 7;
    const size_t go = (size_t)(rh * 64 + row) * 1024 + ct * 128 + chunk * 16;
    u32x4 xv;
    if (ch) {
      unsigned soa = WS_HO + (unsigned)(rh * 72 + rg) * 4096u, sob = 6 * 131072, da = ldw + 6 * SLOT, db = ldw;
      FT(52); BAR(); FT(53);
      auto lda = [&]() { item16s(wsr, voffL, soa, da); soa += 16384; da = da == ldw + 8 * SLOT ? ldw + 6 * SLOT : da + SLOT; };
      auto ldb = [&](rsrc_t rb) { item16(rb, vD, sob, db); sob += 131072; db = db == ldw + 5 * SLOT ? ldw : db + SLOT; };
      lda(); lda(); lda();
      VMW(8); BAR();
      VMW(4); BAR(); lda(); ldb(rw2);
      VMW(8); BAR(); lda(); ldb(rw2);
#pragma clang loop unroll(disable)
      for (int n = 3; n < 9; ++n) { VMW(12); BAR(); lda(); ldb(rw2); }
      const rsrc_t rwo = mkrsrc(wo, D * D * 2);
      sob = 0; vD[1] += 8192; vD[2] -= 8192;
      flags_get(WS_FO + fho);
#pragma clang loop unroll(disable)
      for (int n = 9; n < 12; ++n) { VMW(12); BAR(); lda(); ldb(rwo); }
      VMW(12); BAR(); ldb(rwo); flags_chk(WS_FO + fho); lda();
      VMW(12); BAR(); lda();
      VMW(4); BAR(); lda();
      VMW(4); BAR(); lda();
      xv = *(const u32x4*)(x + go);
      VMW(4); BAR();
      if (b == 0 && w == 7) {
#pragma clang loop unroll(disable)
        for (int c = 0; c < 8; ++c) poll(ws, WS_FO + (c * 64 + l) * 4, (l & 1) ? tag[1] : tag[0]);
        set_flag(tag, wsr, WS_EPOCH);
      }
      VMW(0); BAR();
    } else {
      const int aB = aB0 ^ (64 * nh), aA = 6 * SLOT + mh * 2048 + l * 16;
      int ia = 0, ib = 0;
      s16x4 pb[2][2][2][2]; bf16x8 pa[2][2][2];
#pragma unroll
      for (int s = 0; s < 2; ++s)
#pragma unroll
        for (int n = 0; n < 2; ++n) { pa[1][s][n] = bf16x8{}; pb[1][s][n][0] = pb[1][s][n][1] = s16x4{}; }
      FT(52); BAR(); FT(53);
      STAMP(7);
      xv = *(const u32x4*)(x + go);
      VMW(0);
#pragma clang loop unroll(disable)
      for (int n = 0; n < 18; ++n) {
        BAR();
        if (n == 0 && w == 0) set_flag(tag, wsr, WS_FO + r * 128 + hp * 8);
        const lptr ap = smem + ia + aA, bp = smem + ib + aB, bq = lxor(bp, 32);
#pragma unroll
        for (int hf = 0; hf < 2; ++hf)
#pragma unroll
          for (int s = 0; s < 2; ++s) { const int o_ = (2 * hf + s) * 4096, c = hf ^ 1;
            const bf16x8 f0_ = cat8(pb[c][s][0][0], pb[c][s][0][1]), f1_ = cat8(pb[c][s][1][0], pb[c][s][1][1]);
            pb[hf][s][0][0] = ldtr(bp + o_); pb[hf][s][0][1] = ldtr(bp + o_ + 2048);
            SBAR(); dacc[0][0] = MFMA16(f0_, pa[c][s][0], dacc[0][0]); SBAR();
            pa[hf][s][0] = ld128(ap + o_);
            SBAR(); dacc[0][1] = MFMA16(f1_, pa[c][s][0], dacc[0][1]); SBAR();
            pb[hf][s][1][0] = ldtr(bq + o_); pb[hf][s][1][1] = ldtr(bq + o_ + 2048);
            SBAR(); dacc[1][0] = MFMA16(f0_, pa[c][s][1], dacc[1][0]); SBAR();
            pa[hf][s][1] = ld128(ap + o_ + 1024);
            SBAR(); dacc[1][1] = MFMA16(f1_, pa[c][s][1], dacc[1][1]); SBAR(); }
        ia = ia == 2 * SLOT ? 0 : ia + SLOT; ib = ib == 5 * SLOT ? 0 : ib + SLOT;
      }
#pragma unroll
      for (int s = 0; s < 2; ++s) { const bf16x8 f0_ = cat8(pb[1][s][0][0], pb[1][s][0][1]), f1_ = cat8(pb[1][s][1][0], pb[1][s][1][1]);
        dacc[0][0] = MFMA16(f0_, pa[1][s][0], dacc[0][0]); dacc[0][1] = MFMA16(f1_, pa[1][s][0], dacc[0][1]);
        dacc[1][0] = MFMA16(f0_, pa[1][s][1], dacc[1][0]); dacc[1][1] = MFMA16(f1_, pa[1][s][1], dacc[1][1]); }
#pragma unroll
      for (int m = 0; m < 2; ++m)
#pragma unroll
        for (int n = 0; n < 2; ++n) { const int rw = 32 * mh + 16 * m + t16; *(LAS f32x4*)(smem + rw * 256 + (((8 * nh + 4 * n + g4) ^ (rw & 15)) << 4)) = dacc[m][n]; }
    }
    STAMP(8); FT(54);
    const u32x4 bv = *(LAS u32x4*)(smem + M_B2 + chunk * 16);
    BAR();
    const f32x4 a0 = *(LAS f32x4*)(smem + row * 256 + (((2 * chunk) ^ (row & 15)) << 4)), a1 = *(LAS f32x4*)(smem + row * 256 + (((2 * chunk + 1) ^ (row & 15)) << 4));
    u32x4 ov;
    ov[0] = pk(a0[0] + (bflo(xv[0]) + bflo(bv[0])), a0[1] + (bfhi(xv[0]) + bfhi(bv[0])));
    ov[1] = pk(a0[2] + (bflo(xv[1]) + bflo(bv[1])), a0[3] + (bfhi(xv[1]) + bfhi(bv[1])));
    ov[2] = pk(a1[0] + (bflo(xv[2]) + bflo(bv[2])), a1[1] + (bfhi(xv[2]) + bfhi(bv[2])));
    ov[3] = pk(a1[2] + (bflo(xv[3]) + bflo(bv[3])), a1[3] + (bfhi(xv[3]) + bfhi(bv[3])));
    *(u32x4*)(out + go) = ov;
    STAMP(9); FT(55);
  }
}
}
#if defined(__HIP_DEVICE_COMPILE__)
#pragma clang attribute pop
#endif

extern "C" void kernel_launch(void* const* d_in, const int* in_sizes, int n_in, void* d_out, int out_size, void* d_ws, size_t ws_size, hipStream_t stream) {
  fk::fused<<<256, 512, 0, stream>>>((const char*)d_in[0], (const char*)d_in[1], (const char*)d_in[2], (const char*)d_in[4], (const char*)d_in[5],
                                     (const char*)d_in[6], (const char*)d_in[7], (char*)d_out, (char*)d_ws);
}
```

```cpp
#include <hip/hip_runtime.h>
#include <stdint.h>

#if defined(__HIP_DEVICE_COMPILE__)
#pragma clang attribute push(__attribute__((target("no-packed-fp32-ops"))), apply_to = function)
#endif
namespace fk {
typedef unsigned short u16;
typedef unsigned long long u64;
typedef short bf16x8 __attribute__((ext_vector_type(8)));
typedef short s16x4 __attribute__((ext_vector_type(4)));
typedef float f32x2 __attribute__((ext_vector_type(2)));
typedef float f32x4 __attribute__((ext_vector_type(4)));
typedef float f32x16 __attribute__((ext_vector_type(16)));
typedef unsigned u32x2 __attribute__((ext_vector_type(2)));
typedef unsigned u32x4 __attribute__((ext_vector_type(4)));
typedef __bf16 bf16x2_t __attribute__((ext_vector_type(2)));
#define LAS __attribute__((address_space(3)))
#define GAS __attribute__((address_space(1)))
#define DI __device__ __forceinline__
typedef LAS char* lptr;

constexpr int S = 2048, D = 512, F = 1792;
constexpr unsigned WS_EPOCH = 0;
constexpr unsigned WS_FQKV = 256;
constexpr unsigned WS_FH = WS_FQKV + 16 * 256;
constexpr unsigned WS_FO = WS_FH + 16 * 128;
constexpr unsigned WS_DBG = 16384;
constexpr unsigned WS_QKV = 1u << 20;
constexpr unsigned WS_HO = WS_QKV + 6u * (1u << 20);
constexpr unsigned WS_END = WS_HO + 32u * 72u * 4096u;
constexpr int SLOT = 16384, NSLOT = 9, MISC = NSLOT * SLOT;
constexpr int M_STATS = MISC, M_B1 = MISC + 1024  , M_B2 = MISC + 1536, M_EP = MISC + 1792, M_ONES = MISC + 2048  , M_CNT = MISC + 3072,
              M_TOUCH = MISC + 3328  , M_JUNK = MISC + 3584, M_CS = MISC + 4096  , M_Q = MISC + 8192  ;
constexpr int LDS_BYTES = 163840;

#define MFMA16(a, b, c) __builtin_amdgcn_mfma_f32_16x16x32_bf16((a), (b), (c), 0, 0, 0)
#define MFMA16K16(a, b, c) __builtin_amdgcn_mfma_f32_16x16x16bf16_1k((a), (b), (c), 0, 0, 0)
#define MFMA32(a, b, c) __builtin_amdgcn_mfma_f32_32x32x16_bf16((a), (b), (c), 0, 0, 0)
#define MACC(first, c, a, b) do { if (first) asm volatile("v_mfma_f32_16x16x32_bf16 %0, %1, %2, 0" : "=&v"(c) : "v"(a), "v"(b)); \
                                  else asm volatile("v_mfma_f32_16x16x32_bf16 %0, %1, %2, %0" : "+v"(c) : "v"(a), "v"(b)); } while (0)
#define MEND() asm volatile("s_nop 7" : "+v"(acc[0][0]), "+v"(acc[0][1]), "+v"(acc[0][2]), "+v"(acc[0][3]), "+v"(acc[1][0]), "+v"(acc[1][1]), "+v"(acc[1][2]), "+v"(acc[1][3]))
#define PRIO(n) __builtin_amdgcn_s_setprio(n)
#define SBAR() __builtin_amdgcn_sched_barrier(0)
#define BAR() asm volatile("s_waitcnt lgkmcnt(0)\n\ts_barrier" ::: "memory")
#define VMW(N) asm volatile("s_waitcnt vmcnt(" #N ")" ::: "memory")
#define RLX_AGENT __ATOMIC_RELAXED, __HIP_MEMORY_SCOPE_AGENT

typedef __amdgpu_buffer_rsrc_t rsrc_t;
DI unsigned rfl(unsigned v) { return __builtin_amdgcn_readfirstlane(v); }
DI rsrc_t mkrsrc(const void* p, unsigned bytes) { return __builtin_amdgcn_make_buffer_rsrc((void*)p, 0, bytes, 0x00020000); }
DI void setm0(unsigned lds_dst) { asm volatile("s_mov_b32 m0, %0\n\ts_nop 0" ::"s"(rfl(lds_dst)) : "memory"); }
template <int OFF> DI void pc16(rsrc_t rs, unsigned voff, unsigned soff) {
  asm volatile("buffer_load_dwordx4 %0, %1, %2 offen offset:%3 lds" ::"v"(voff), "s"(rs), "s"(rfl(soff)), "i"(OFF) : "memory"); }
template <int OFF> DI void pc16s(rsrc_t rs, unsigned voff, unsigned soff) {
  asm volatile("buffer_load_dwordx4 %0, %1, %2 offen offset:%3 sc1 lds" ::"v"(voff), "s"(rs), "s"(rfl(soff)), "i"(OFF) : "memory"); }
DI void item16(rsrc_t rs, const unsigned (&v)[4], unsigned soff, unsigned dst) { setm0(dst); pc16<0>(rs, v[0], soff); pc16<1024>(rs, v[1], soff); pc16<2048>(rs, v[2], soff); pc16<3072>(rs, v[3], soff); }
DI void item16s(rsrc_t rs, unsigned v, unsigned soff, unsigned dst) { setm0(dst); pc16s<0>(rs, v, soff); pc16s<1024>(rs, v, soff); pc16s<2048>(rs, v, soff); pc16s<3072>(rs, v, soff); }
DI void dma4(rsrc_t rs, unsigned voff, unsigned lds_dst) { setm0(lds_dst); asm volatile("buffer_load_dword %0, %1, 0 offen lds" ::"v"(voff), "s"(rs) : "memory"); }
DI void dma4_sc1(rsrc_t rs, unsigned voff, unsigned lds_dst) { setm0(lds_dst); asm volatile("buffer_load_dword %0, %1, 0 offen sc1 lds" ::"v"(voff), "s"(rs) : "memory"); }
DI void st16_sc1(u32x4 v, rsrc_t rs, unsigned voff, unsigned soff) {
  asm volatile("buffer_store_dwordx4 %0, %1, %2, %3 offen sc1\n\ts_nop 1" ::"v"(v), "v"(voff), "s"(rs), "s"(rfl(soff)) : "memory"); }
DI void set_flag(u32x2 tag, rsrc_t rs, unsigned soff) {
  asm volatile("s_mov_b64 exec, 1\n\tbuffer_store_dwordx2 %0, off, %1, %2 sc1\n\ts_mov_b64 exec, -1" ::"v"(tag), "s"(rs), "s"(rfl(soff)) : "memory"); }
DI void count_flag(unsigned cnt_addr, unsigned last, u32x2 tag, rsrc_t rs, unsigned soff) {
  unsigned t;
  asm volatile("s_mov_b64 exec, 1\n\tv_mov_b32 %0, 1\n\tds_add_rtn_u32 %0, %1, %0\n\ts_waitcnt lgkmcnt(0)\n\tv_cmp_eq_u32 vcc, %2, %0\n\ts_cbranch_vccz 1f\n\t"
               "buffer_store_dwordx2 %3, off, %4, %5 sc1\n1:\n\ts_mov_b64 exec, -1"
               : "=&v"(t) : "v"(cnt_addr), "i"(last), "v"(tag), "s"(rs), "s"(rfl(soff)) : "vcc", "memory"); }

DI bf16x8 ld128(lptr p) { return *(LAS bf16x8*)p; }
DI s16x4 ldtr(lptr p) { return __builtin_amdgcn_ds_read_tr16_b64_v4i16((LAS s16x4*)p); }
DI bf16x8 cat8(s16x4 a, s16x4 b) { return __builtin_shufflevector(a, b, 0, 1, 2, 3, 4, 5, 6, 7); }
DI bf16x8 ldtr2(lptr p, int second) { return cat8(ldtr(p), ldtr(p + second)); }
DI unsigned pk(float lo, float hi) { f32x2 f = {lo, hi}; return __builtin_bit_cast(unsigned, __builtin_convertvector(f, bf16x2_t)); }
DI float u2f(unsigned u) { return __builtin_bit_cast(float, u); }
DI unsigned f2u(float f) { return __builtin_bit_cast(unsigned, f); }
DI float bflo(unsigned u) { return u2f(u << 16); }
DI float bfhi(unsigned u) { return u2f(u & 0xffff0000u); }
DI unsigned split2(float v) { const unsigned h = pk(v, 0.f); return pk(v, v - bflo(h)); }
DI lptr lxor(lptr p, unsigned m) { return (lptr)(uintptr_t)((unsigned)(uintptr_t)p ^ m); }
DI bool all_lanes(bool c) { return __builtin_amdgcn_ballot_w64(!c) == 0; }

DI void poll(const char* ws, unsigned off, unsigned want) {
  GAS unsigned* p = (GAS unsigned*)(ws + off);
#pragma clang loop unroll(disable)
  for (unsigned spins = 0; spins < (1u << 16); ++spins) {
    if (all_lanes(__hip_atomic_load(p, RLX_AGENT) == want)) break;
    __builtin_amdgcn_s_sleep(2);
  }
  asm volatile("" ::: "memory");
}

#define FT(i) do { } while (0)
#define STAMP(i) do { } while (0)

__global__ __launch_bounds__(512, 2) void fused(const char* __restrict__ x, const char* __restrict__ wqkv, const char* __restrict__ wo,
                                                const char* __restrict__ w1, const char* __restrict__ b1, const char* __restrict__ w2,
                                                const char* __restrict__ b2, char* __restrict__ out, char* ws) {
  __shared__ __attribute__((aligned(1024))) char smem_[LDS_BYTES];
  const lptr smem = (lptr)smem_;
  const unsigned lds0 = (unsigned)(uintptr_t)smem;
  const int tid = __builtin_amdgcn_workitem_id_x(), l = tid & 63;
  const int w = __builtin_amdgcn_readfirstlane(tid >> 6);
  const int b = __builtin_amdgcn_workgroup_id_x(), xcd = b & 7, jj = b >> 3;
  const int r = (xcd & 1) * 8 + (jj & 7), type = jj >> 3, uq = xcd >> 1;
  const int hp = uq * 4 + type;
  const int rh = xcd * 4 + (jj & 3), ct = jj >> 2;
  const bool q1 = type < 2;
  const int kind0 = 1 + (type >> 1), hq = 2 * uq + (type & 1);
  const int f2 = uq * 7 + (q1 ? type : 2 * type - 1), f3 = uq * 7 + 6;
  const int rg = w & 3, ch = w >> 2, m0 = type & 1;
  STAMP(0); FT(0);
  const unsigned colsw = (unsigned)(((((l & 7) >> 1) ^ ((l >> 4) & 3)) << 5) + ((l & 1) << 4));
  const unsigned rowW = (unsigned)(8 * (l >> 5) + ((l >> 3) & 3) + 32 * rg);
  const rsrc_t r1 = mkrsrc(w1, D * F * 2), wsr = mkrsrc(ws, WS_END);
  const unsigned ldw = lds0 + rg * 4096;
  const int t16 = l & 15, g4 = l >> 4;
  const int aB0 = (4 * g4 + ((l >> 2) & 3)) * 128 + 8 * (l & 3) + ((((g4 & 1) << 1) | ((l >> 3) & 1)) << 5);
  const int aX0 = (32 * rg + 16 * m0 + t16) * 128 + ((g4 ^ (t16 >> 1)) << 4);
  bf16x8 ones = {0x3f80, 0x3f80, 0x3f80, 0x3f80, 0x3f80, 0x3f80, 0x3f80, 0x3f80};
  bf16x8 xf[2][16];
  f32x4 acc[2][4], cs[4];
  s16x4 h[2][4][2], cor[2];
  float rstd_[2], std_[2];
  u32x2 tag;
  unsigned vF[4], vD0, fkv;
  int ac[4];
  auto setup = [&]() {
#pragma unroll
    for (int c = 0; c < 4; ++c) { vF[c] = (rowW + 16 * (c & 1) + 4 * (c >> 1)) * 3584u + colsw - 1024u * c; ac[c] = (aB0 ^ (32 * c)) + rg * 4096;
      asm volatile("" : "+v"(vF[c]), "+v"(ac[c])); }
    vD0 = (unsigned)((32 * rg + (l >> 3)) * 1024 + ct * 128) + colsw;
    int fr = r - 2 + ((l >> 2) & 3); fr = fr < 0 ? 0 : fr > r ? r : fr;
    fkv = WS_FQKV + fr * 256 + (hp >> 1) * 24 + 8 + (l & 3) * 4;
    asm volatile("" : "+v"(ones), "+v"(vD0), "+v"(fkv)); };
  auto cs_item = [&](int off) {
    bf16x8 f[4];
#pragma unroll
    for (int n = 0; n < 4; ++n) f[n] = ldtr2(smem + ac[n] + off, 2048);
    SBAR(); PRIO(2);
#pragma unroll
    for (int n = 0; n < 4; ++n) cs[n] = MFMA16(ones, f[n], cs[n]);
    SBAR(); PRIO(0); };
  auto cs_move = [&](int d) {
#pragma unroll
    for (int n = 0; n < 4; ++n) { ac[n] += d; asm volatile("" : "+v"(ac[n])); } };
  auto put_cs = [&](int buf, unsigned bv) {
    const float c0 = cs[0][0], c1 = cs[1][0], c2 = cs[2][0], c3 = cs[3][0];
    const float lo = (g4 & 1) ? c1 : c0, hi = (g4 & 1) ? c3 : c2;
    const unsigned hl = split2((g4 & 2) ? hi : lo);
    *(LAS u32x2*)(smem + M_CS + buf * 2048 + rg * 512 + l * 8) = u32x2{hl, (hl & 0xffffu) | (bv << 16)}; };
  auto bias16 = [&](int boff, bool on) -> unsigned { const unsigned v = *(LAS u16*)(smem + M_B1 + boff + 2 * l); return on ? v : 0u; };
  auto fix_acc = [&]() {
    s16x4 a[4];
#pragma unroll
    for (int n = 0; n < 4; ++n) a[n] = *(LAS s16x4*)(smem + M_CS + ch * 2048 + g4 * 512 + (16 * n + t16) * 8);
    SBAR(); PRIO(2);
#pragma unroll
    for (int n = 0; n < 4; ++n) { acc[0][n] = MFMA16K16(a[n], cor[0], acc[0][n]); acc[1][n] = MFMA16K16(a[n], cor[1], acc[1][n]); }
    SBAR(); PRIO(0); };
  auto flags_get = [&](unsigned off) { dma4_sc1(wsr, off, lds0 + M_Q + w * 1024); };
  auto flags_chk = [&](unsigned off) { const unsigned want = (l & 1) ? tag[1] : tag[0];
    if (__builtin_expect(!all_lanes(*(LAS unsigned*)(smem + M_Q + w * 1024 + l * 4) == want), 0)) poll(ws, off, want); };
  auto qkv_head = [&](int n, float rs0, float rs1, unsigned lo, unsigned so) {
    const f32x4 y0 = acc[0][n] * rs0, y1 = acc[1][n] * rs1;
    auto s0 = __builtin_amdgcn_permlane16_swap(pk(y0[0], y0[1]), pk(y1[0], y1[1]), false, false);
    auto s1 = __builtin_amdgcn_permlane16_swap(pk(y0[2], y0[3]), pk(y1[2], y1[3]), false, false);
    st16_sc1(u32x4{s0[0], s1[0], s0[1], s1[1]}, wsr, lo, so + n * 65536u); };
  auto gelu8 = [&](f32x4 a, f32x4 c, int m) -> u32x4 {
    const float c1 = -2.f * 0.7978845608028654f * 1.4426950408889634f, k1 = c1 * rstd_[m], k2 = (c1 * 0.044715f) * rstd_[m] * rstd_[m] * rstd_[m], sd = std_[m];
    float y[8];
#pragma unroll
    for (int e = 0; e < 8; ++e) { const float v = e < 4 ? a[e & 3] : c[e & 3];
      const float ex = __builtin_amdgcn_exp2f(v * __builtin_fmaf(v * v, k2, k1));
      y[e] = v * __builtin_amdgcn_rcpf(__builtin_fmaf(ex, sd, sd)); }
    return u32x4{pk(y[0], y[1]), pk(y[2], y[3]), pk(y[4], y[5]), pk(y[6], y[7])}; };
  auto get_stats = [&]() {
    const u32x2 ep = *(LAS u32x2*)(smem + M_EP);
    const u64 t = (((u64)ep[1] << 32) | ep[0]) + 1; tag = u32x2{(unsigned)t, (unsigned)(t >> 32)};
#pragma unroll
    for (int m = 0; m < 2; ++m) { const f32x2 s2 = *(LAS f32x2*)(smem + M_STATS + (32 * rg + 16 * m + t16) * 8);
      const float mean = s2[0] * (1.f / D), ve = __builtin_fmaxf(__builtin_fmaf(s2[1], 1.f / D, -mean * mean), 0.f) + 1e-5f;
      rstd_[m] = __builtin_amdgcn_rsqf(ve); std_[m] = ve * rstd_[m];
      const unsigned mm = split2(-mean), ss = split2(std_[m]);
      cor[m] = __builtin_bit_cast(s16x4, u32x2{(mm & 0xffffu) | (mm << 16), (mm >> 16) | (g4 == 0 ? ss << 16 : ss & 0xffff0000u)}); } };
  const unsigned sF2 = (unsigned)f2 * 128u, sF3 = (unsigned)f3 * 128u;
  constexpr unsigned QF = 128 * 3584;

  if (ch) {
    __builtin_amdgcn_s_setprio(2);
    unsigned vQ[4], vX[4];
#pragma unroll
    for (int c = 0; c < 4; ++c) { vX[c] = (unsigned)((32 * rg + (l >> 3)) * 1024 + c * 7168 + (((l & 7) ^ ((4 * (c & 1) + (l >> 4)) & 7)) << 4));
      vQ[c] = (rowW + 16 * (c & 1) + 4 * (c >> 1)) * 3072u + colsw - 1024u * c; }
    const rsrc_t rx = mkrsrc(x, S * D * 2), rq = mkrsrc(wqkv, D * 3 * D * 2);
    const unsigned c0 = (unsigned)(kind0 * 1024 + hq * 128), xo = (unsigned)r * 131072u;
    auto trip = [&](int u, int i) { const int s0 = (3 * u) % NSLOT;
      if (i < 2) item16(rx, vX, xo + u * 256 + i * 128, ldw + (s0 + i) * SLOT); else item16(rq, vQ, c0 + u * (128 * 3072), ldw + (s0 + 2) * SLOT); };
    trip(0, 0); trip(0, 2); trip(0, 1); trip(1, 0); trip(1, 2); trip(1, 1);
    setup();
    const rsrc_t rs1 = mkrsrc(q1 ? wqkv : w1, q1 ? D * 3 * D * 2 : D * F * 2);
    const unsigned c1 = (unsigned)(q1 ? hq : f2 - 1) * 128u, st1 = q1 ? 128 * 3072 : QF;
    f32x4 sm[2] = {}, sq[2] = {};
    cs[0] = cs[1] = cs[2] = cs[3] = f32x4{};
    int xb4[4];
#pragma unroll
    for (int n = 0; n < 4; ++n) { xb4[n] = aX0 ^ (64 * (n >> 1)) ^ (2048 * (n & 1)); asm volatile("" : "+v"(xb4[n])); }
#pragma unroll
    for (int u = 0; u < 4; ++u) {
      if (u > 0) { const int d = u == 3 ? -6 * SLOT : 3 * SLOT; cs_move(d);
#pragma unroll
        for (int n = 0; n < 4; ++n) { xb4[n] += d; asm volatile("" : "+v"(xb4[n])); } }
      if (u == 2) {
#pragma unroll
        for (int c = 0; c < 4; ++c) vQ[c] = q1 ? vQ[c] : vF[c]; }
      FT(1 + 3 * u); VMW(12); BAR(); FT(3 + 3 * u);
#pragma unroll
      for (int c = 0; c < 4; ++c) {
        const int k = 4 * u + c;
        xf[0][k] = ld128(smem + xb4[2 * (c & 1)] + (c >> 1) * SLOT); xf[1][k] = ld128(smem + xb4[2 * (c & 1) + 1] + (c >> 1) * SLOT);
        const bf16x8 cf = ldtr2(smem + 2 * SLOT + ac[c], 2048);
        SBAR();
        if (c < 3) {
          if (u < 2) trip(u + 2, c);
          if (u == 2) item16(rs1, vQ, c1 + c * st1, ldw + (3 + c) * SLOT);
          if (u == 3) { if (c == 0) item16(rs1, vQ, c1 + 3 * st1, ldw + 6 * SLOT); else item16(r1, vF, sF2 + (c - 1) * QF, ldw + (6 + c) * SLOT); }
        }
        sm[0] = MFMA16(ones, xf[0][k], sm[0]); sq[0] = MFMA16(xf[0][k], xf[0][k], sq[0]);
        sm[1] = MFMA16(ones, xf[1][k], sm[1]); sq[1] = MFMA16(xf[1][k], xf[1][k], sq[1]);
        cs[c] = MFMA16(ones, cf, cs[c]);
        SBAR();
      }
    }
    { const bool e0 = l & 1, e1 = l & 2;
      const float d0 = e1 ? (e0 ? sq[0][3] : sq[0][2]) : (e0 ? sq[0][1] : sq[0][0]), d1 = e1 ? (e0 ? sq[1][3] : sq[1][2]) : (e0 ? sq[1][1] : sq[1][0]);
      const lptr sp = smem + (g4 == (t16 >> 2) ? M_STATS + (32 * rg + t16) * 8 : M_Q + w * 1024);
      *(LAS f32x2*)sp = f32x2{sm[0][0], d0}; *(LAS f32x2*)(sp + 128) = f32x2{sm[1][0], d1}; }
    put_cs(0, 0);
    __builtin_amdgcn_s_setprio(0);
    FT(13); VMW(12);
  } else {
    { u64 pc; unsigned csz; asm volatile("s_getpc_b64 %0\nLfk_pc:\n\ts_mov_b32 %1, Lfk_end-Lfk_pc" : "=s"(pc), "=s"(csz));
      if (w == 0) dma4(mkrsrc(x, S * D * 2), (unsigned)((r * 16 + hp) * 64 + l) * 128u, lds0 + M_TOUCH);
      if (w == 2) dma4(mkrsrc(wqkv, D * 3 * D * 2), (unsigned)(b * 48 + (l < 48 ? l : 0)) * 128u, lds0 + M_TOUCH);
      if (w == 3) dma4(mkrsrc((const void*)pc, csz), (unsigned)(jj * 8 + (l & 7)) * 128u, lds0 + M_TOUCH); }
    if (w == 0) dma4_sc1(wsr, (unsigned)(l & 1) * 4u, lds0 + M_EP);
    if (w == 1) { const rsrc_t rb = mkrsrc(b1, F * 2); dma4(rb, (unsigned)(l * 4 + (f2 - 1) * 128), lds0 + M_B1); dma4(rb, (unsigned)(l * 4 + f3 * 128), lds0 + M_B1 + 256); }
    if (w == 2) dma4(mkrsrc(b2, D * 2), (unsigned)(l * 4 + ct * 128), lds0 + M_B2);
    if (w == 3) { *(LAS u32x4*)(smem + M_ONES + l * 16) = u32x4{0x3f803f80u, 0x3f803f80u, 0x3f803f80u, 0x3f803f80u}; *(LAS unsigned*)(smem + M_CNT + l * 4) = 0u; }
    setup();
    int wb4[4], xb4[4];
#pragma unroll
    for (int n = 0; n < 4; ++n) { wb4[n] = (aB0 ^ (32 * n)) + 2 * SLOT; xb4[n] = aX0 ^ (64 * (n >> 1)) ^ (2048 * (n & 1)); asm volatile("" : "+v"(wb4[n]), "+v"(xb4[n])); }
    FT(2); BAR(); FT(3);
    xf[0][0] = ld128(smem + xb4[0]); xf[1][0] = ld128(smem + xb4[1]);
#pragma unroll
    for (int n = 0; n < 4; ++n) { h[0][n][0] = ldtr(smem + wb4[n]); h[0][n][1] = ldtr(smem + wb4[n] + 2048); }
#pragma unroll
    for (int k = 0; k < 16; ++k) {
      const int kn = k + 1, c = k & 1;
      if ((k & 3) == 3 && k < 15) {
#pragma unroll
        for (int n = 0; n < 4; ++n) { const int d = k == 11 ? -6 * SLOT : 3 * SLOT; wb4[n] += d; xb4[n] += d; asm volatile("" : "+v"(wb4[n]), "+v"(xb4[n])); }
        if (k == 11) VMW(0);
        if (k == 3 && w == 1) dma4(r1, (unsigned)(b * 56 + (l < 56 ? l : 0)) * 128u, lds0 + M_TOUCH);
        if (k == 11 && w >= 2) { const int nl = w == 2 ? 56 : 16; dma4(mkrsrc(w == 2 ? w2 : wo, nl * 32768), (unsigned)(b * nl + (l < nl ? l : 0)) * 128u, lds0 + M_TOUCH); }
        FT(2 + 3 * (kn >> 2)); BAR(); FT(3 + 3 * (kn >> 2)); }
#pragma unroll
      for (int n = 0; n < 4; ++n) {
        const bf16x8 bf = cat8(h[c][n][0], h[c][n][1]);
        if (k < 15) { h[c ^ 1][n][0] = ldtr(smem + wb4[n] + (kn & 3) * 4096); if (n < 2) xf[n][kn] = ld128(smem + xb4[2 * (kn & 1) + n] + ((kn & 3) >> 1) * SLOT); }
        SBAR(); MACC(k == 0, acc[0][n], bf, xf[0][k]); SBAR();
        if (k < 15) h[c ^ 1][n][1] = ldtr(smem + wb4[n] + (kn & 3) * 4096 + 2048);
        SBAR(); MACC(k == 0, acc[1][n], bf, xf[1][k]); SBAR();
      }
    }
    MEND();
  }
  const int tok = (l & 31) ^ (16 * m0);
  const unsigned qso = WS_QKV + (unsigned)(hq * 256 + 4 * r + rg) * 1024u;
  FT(14); BAR(); FT(15);
  STAMP(1);

  if (ch == 0) {
    item16(r1, vF, sF2 + 2 * QF, ldw);
    get_stats();
    { const u32x2 c0 = __builtin_bit_cast(u32x2, cor[0]), c1 = __builtin_bit_cast(u32x2, cor[1]);
      *(LAS u32x4*)(smem + M_Q + rg * 1024 + l * 16) = u32x4{c0[0], c0[1], c1[0], c1[1]};
      *(LAS f32x4*)(smem + M_Q + 4096 + rg * 1024 + l * 16) = f32x4{rstd_[0], rstd_[1], std_[0], std_[1]};
      *(LAS u32x2*)(smem + M_EP + 8) = tag; }
    cs[0] = cs[1] = cs[2] = cs[3] = f32x4{};
    cs_move(3 * SLOT);
    FT(16); fix_acc(); SBAR();
    item16(r1, vF, sF2 + 3 * QF, ldw + SLOT);
    cs_item(0); SBAR();
    const unsigned lo = kind0 == 2 ? tok * 32 + (l >> 5) * 16 : (l >> 5) * 512 + tok * 16, so = qso + kind0 * (32u * 65536u);
    qkv_head(0, rstd_[0], rstd_[1], lo, so); qkv_head(1, rstd_[0], rstd_[1], lo, so); SBAR();
    cs_item(SLOT); SBAR();
    FT(20); BAR(); FT(21);
    item16(r1, vF, sF3, ldw + 2 * SLOT);
    qkv_head(2, rstd_[0], rstd_[1], lo, so); qkv_head(3, rstd_[0], rstd_[1], lo, so); SBAR();
    item16(r1, vF, sF3 + QF, ldw + 3 * SLOT);
    const unsigned bv = bias16(0, !q1 && rg < 2);
    cs_item(2 * SLOT); SBAR();
    item16(r1, vF, sF3 + 2 * QF, ldw + 4 * SLOT);
    cs_item(3 * SLOT);
    put_cs(1, bv);
    VMW(8);
    FT(24); BAR(); FT(25);
  }
  {
    const int sa = ch ? 3 : 7, sb = ch ? 5 : 0;
    int a4[4];
    PRIO(1);
#pragma unroll
    for (int n = 0; n < 4; ++n) { a4[n] = (aB0 ^ (32 * n)) + sa * SLOT; h[0][n][0] = ldtr(smem + a4[n]); h[0][n][1] = ldtr(smem + a4[n] + 2048); }
#pragma unroll
    for (int k = 0; k < 16; ++k) {
      const int kn = k + 1, c = k & 1;
      if (k == 7) { FT(26); VMW(0); BAR(); FT(27);
#pragma unroll
        for (int n = 0; n < 4; ++n) a4[n] += (sb - sa) * SLOT; }
#pragma unroll
      for (int n = 0; n < 4; ++n) {
        const bf16x8 bf = cat8(h[c][n][0], h[c][n][1]);
        if (k < 15) h[c ^ 1][n][0] = ldtr(smem + a4[n] + (kn & 7) * 4096);
        SBAR(); MACC(k == 0, acc[0][n], bf, xf[0][k]); SBAR();
        if (k < 15) h[c ^ 1][n][1] = ldtr(smem + a4[n] + (kn & 7) * 4096 + 2048);
        SBAR(); MACC(k == 0, acc[1][n], bf, xf[1][k]); SBAR();
      }
    }
    MEND();
    PRIO(0);
  }
  if (ch) { const u32x4 c4 = *(LAS u32x4*)(smem + M_Q + rg * 1024 + l * 16); const f32x4 s4 = *(LAS f32x4*)(smem + M_Q + 4096 + rg * 1024 + l * 16);
    cor[0] = __builtin_bit_cast(s16x4, u32x2{c4[0], c4[1]}); cor[1] = __builtin_bit_cast(s16x4, u32x2{c4[2], c4[3]});
    rstd_[0] = s4[0]; rstd_[1] = s4[1]; std_[0] = s4[2]; std_[1] = s4[3]; tag = *(LAS u32x2*)(smem + M_EP + 8); }
  const unsigned voffL = (unsigned)l * 16u;
  const rsrc_t rw2 = mkrsrc(w2, F * D * 2);
  const unsigned qsrc = WS_QKV + 2 * hp * 65536;
  const unsigned fq = WS_FQKV + r * 256 + (hp >> 1) * 24 + (l & 1) * 4;
  const unsigned hbase = WS_HO + (unsigned)((2 * r + (rg >> 1)) * 288 + 2 * (rg & 1) + m0) * 1024u;
  STAMP(2);
  FT(28); BAR(); FT(29);
  {
    const bool isq = ch && q1;
    const unsigned hs = hbase + (unsigned)(f2 - ch) * 8192u;
    const unsigned lo = (l >> 5) * 512 + tok * 16;
    const float sc = 0.25f * 1.4426950408889634f;
    if (ch) { if (w == 4) set_flag(tag, wsr, WS_FQKV + r * 256 + (hq * 3 + kind0) * 8);
      item16(r1, vF, sF3 + 3 * QF, ldw + 5 * SLOT); cs_move(7 * SLOT); cs[0] = cs[1] = cs[2] = cs[3] = f32x4{}; }
    else flags_get(fq);
    fix_acc(); SBAR();
    FT(30);
    if (isq) { const float rs0 = rstd_[0] * sc, rs1 = rstd_[1] * sc;
#pragma unroll
      for (int n = 0; n < 4; ++n) qkv_head(n, rs0, rs1, lo, qso); }
    else { st16_sc1(gelu8(acc[0][0], acc[0][1], 0), wsr, voffL, hs); SBAR(); st16_sc1(gelu8(acc[1][0], acc[1][1], 1), wsr, voffL, hs ^ 1024u); }
    SBAR(); FT(31);
    if (ch) {
      cs_item(0); cs_item(SLOT); cs_move(-7 * SLOT);
      flags_get(fkv);
      if (q1) VMW(1);
      FT(36); BAR(); FT(37);
      if (q1 && w == 4) set_flag(tag, wsr, WS_FQKV + r * 256 + hq * 24);
      cs_item(0); SBAR();
      VMW(1); if (w != 4 || !q1) VMW(0);
      flags_chk(fkv);
#pragma unroll
      for (int j = 0; j < 3; ++j) { int rr = r - 2 + j; rr = rr < 0 ? 0 : rr;
        item16s(wsr, voffL, qsrc + ((rg & 1) + 32 + 32 * (rg >> 1)) * 65536 + rr * 4096, lds0 + 6 * SLOT + rg * 12288 + j * 4096);
        if (j == 0) cs_item(SLOT);
        if (j == 1) put_cs(0, bias16(128, rg < 2));
        SBAR(); }
    } else { VMW(2); flags_chk(fq); setm0(lds0 + M_Q + w * 1024); pc16s<0>(wsr, voffL, qsrc + (4 * r + rg) * 1024); }
    FT(39);
    if (!isq) { st16_sc1(gelu8(acc[0][2], acc[0][3], 0), wsr, voffL, hs + 4096); SBAR(); st16_sc1(gelu8(acc[1][2], acc[1][3], 1), wsr, voffL, (hs + 4096) ^ 1024u); }
    if (ch) { FT(44); BAR(); FT(45); }
  }
  unsigned vD[4] = {vD0, vD0 + 7168, vD0 + 2 * 7168, vD0 + 3 * 7168};
  if (ch) {
    flags_get(fq);
    PRIO(1);
    FT(46);
    const lptr pa = smem + 2 * SLOT + (aB0 ^ (64 * (type >> 1))), pb = lxor(pa, 32);
    const lptr bp = smem + M_B1 + 256 + 64 * (type >> 1) + 2 * t16;
    unsigned ba = *(LAS u16*)bp, bb = *(LAS u16*)(bp + 32);
    f32x4 qa, qb, ca, cb;
    s16x4 g[3][4];
#pragma unroll
    for (int k = 0; k < 2; ++k) { g[k][0] = ldtr(pa + k * 4096); g[k][1] = ldtr(pa + k * 4096 + 2048); g[k][2] = ldtr(pb + k * 4096); g[k][3] = ldtr(pb + k * 4096 + 2048); }
#pragma unroll
    for (int k = 0; k < 16; ++k) {
      const bf16x8 ga = cat8(g[k % 3][0], g[k % 3][1]), gb_ = cat8(g[k % 3][2], g[k % 3][3]);
      const int kn = k + 2, c = kn % 3;
      if (k == 1 || k == 5) item16(rw2, vD, (k >> 2) * 131072, ldw + (k >> 2) * SLOT);
      if (k == 10) { VMW(8); flags_chk(fq); setm0(lds0 + M_Q + w * 1024); pc16s<0>(wsr, voffL, qsrc + 65536 + (4 * r + rg) * 1024); }
      if (kn < 16) g[c][0] = ldtr(pa + kn * 4096);
      SBAR(); MACC(k == 0, qa, ga, xf[0][k]); SBAR();
      if (kn < 16) g[c][1] = ldtr(pa + kn * 4096 + 2048);
      SBAR(); MACC(k == 0, qb, gb_, xf[0][k]); SBAR();
      if (kn < 16) g[c][2] = ldtr(pb + kn * 4096);
      SBAR(); MACC(k == 0, ca, ones, ga); SBAR();
      if (kn < 16) g[c][3] = ldtr(pb + kn * 4096 + 2048);
      SBAR(); MACC(k == 0, cb, ones, gb_); SBAR();
    }
    asm volatile("s_nop 7" : "+v"(qa), "+v"(qb), "+v"(ca), "+v"(cb));
    FT(47);
    ba = l < 32 ? ba << 16 : 0; bb = l < 32 ? bb << 16 : 0;
    const unsigned ha = split2(l < 16 ? ca[0] : 0.f), hb = split2(l < 16 ? cb[0] : 0.f);
    qa = MFMA16K16(__builtin_bit_cast(s16x4, u32x2{ha, (ha & 0xffffu) | ba}), cor[0], qa);
    qb = MFMA16K16(__builtin_bit_cast(s16x4, u32x2{hb, (hb & 0xffffu) | bb}), cor[0], qb);
    st16_sc1(gelu8(qa, qb, 0), wsr, voffL, hbase + (unsigned)f3 * 8192u + (type >> 1) * 4096);
    FT(48); VMW(1);
  } else VMW(2);
  const unsigned fho = (unsigned)((rh >> 1) * 128 + (l & 31) * 4);
  unsigned soa = WS_HO + (unsigned)(rh * 72 + rg) * 4096u;
  bf16x8 br[24];
  {
    const int e = ch, i = rg, hh = l >> 5, iq = l & 31;
    const lptr kp = smem + 6 * SLOT + e * 12288 + i * 1024 + l * 16;
    int bstart = 8 - 4 * r - i; bstart = bstart < 0 ? 0 : bstart;
    unsigned mk[8];
    { const int z = iq - 4 * hh;
#pragma unroll
      for (int c = 0; c < 8; ++c) { const int j0 = 2 * (c & 1) + 8 * (c >> 1); mk[c] = j0 + 1 <= z ? 0xffffffffu : j0 <= z ? 0xffffu : 0u; } }
    const bool onesl = (l & 16) != 0;
    const lptr vp8 = onesl ? smem + M_ONES + 8 * t16 : smem + 6 * SLOT + 24576 + e * 12288 + (i + 8) * 1024 + (4 * hh + (t16 >> 2)) * 32 + (l & 3) * 8;
    const int vstep = onesl ? 0 : 1024;
    const unsigned ovo = (unsigned)((iq >> 4) * 1024 + ((iq & 15) + 16 * (2 * e + hh)) * 16);
    STAMP(3);
    FT(49); BAR(); FT(50);
    __builtin_amdgcn_s_setprio(0);
    STAMP(5);
    const bf16x8 qf = ld128(smem + M_Q + w * 1024 + l * 16), k8 = ld128(kp + 8 * 1024);
    bf16x8 kn = ld128(kp + 7 * 1024);
    const f32x16 zero = {};
    float m;
    { const f32x16 s = MFMA32(k8, qf, zero); m = u2f(__builtin_amdgcn_permlane32_swap(f2u(s[0]), f2u(s[0]), false, false)[0]); }
    f32x16 o, nm;
#pragma unroll
    for (int c = 0; c < 16; ++c) nm[c] = -m;
    lptr vp = vp8;
    f32x16 sc = MFMA32(k8, qf, nm);
#pragma unroll
    for (int bb = 8; bb >= 0; --bb) {
      const bf16x8 v0 = ldtr2(vp, 256), v1 = ldtr2(vp + 512, 256);
      f32x16 sn;
      if (bb > 0) sn = MFMA32(kn, qf, nm);
      if (bb > 1) kn = ld128(kp + (bb - 2) * 1024);
      vp -= vstep;
      if (bb == 8) item16(rw2, vD, (2 + 2 * ch) * 131072, ldw + (2 + 2 * ch) * SLOT);
      if (bb == 6) { VMW(4); count_flag(lds0 + M_CNT, 7, tag, wsr, WS_FH + r * 128 + hp * 8); item16(rw2, vD, (3 + 2 * ch) * 131072, ldw + (3 + 2 * ch) * SLOT); }
      if (bb == 4 && ch == 0) { int a0 = aB0 ^ (32 * rg), a1 = a0 + 3 * SLOT; asm volatile("" : "+v"(a0), "+v"(a1));
#pragma unroll
        for (int k = 0; k < 24; ++k) br[k] = ldtr2(smem + (k < 12 ? a0 : a1) + (k % 12) * 4096, 2048); }
      if (bb == 3) { if (ch) flags_get(WS_FH + fho); item16(rw2, vD, (6 + 3 * ch) * 131072, ldw + 3 * ch * SLOT); }
      if (bb == 2) { if (ch) { VMW(4); flags_chk(WS_FH + fho); item16s(wsr, voffL, soa, ldw + 4 * SLOT); } else item16(rw2, vD, 7 * 131072, ldw + SLOT); }
      if (bb == 1) { if (ch) item16s(wsr, voffL, soa + 16384, ldw + 5 * SLOT); else item16(rw2, vD, 8 * 131072, ldw + 2 * SLOT); }
      SBAR();
      if (bb == 8 || bb >= bstart) {
        unsigned p[8];
#pragma unroll
        for (int c = 0; c < 8; ++c) { p[c] = pk(__builtin_amdgcn_exp2f(sc[2 * c]), __builtin_amdgcn_exp2f(sc[2 * c + 1]));
          if (bb == 8) p[c] &= mk[c];
          if (bb == 0) p[c] &= ~mk[c];
          if (c == 3) o = MFMA32(v0, __builtin_bit_cast(bf16x8, u32x4{p[0], p[1], p[2], p[3]}), bb == 8 ? zero : o); }
        o = MFMA32(v1, __builtin_bit_cast(bf16x8, u32x4{p[4], p[5], p[6], p[7]}), o);
        asm volatile("" : "+v"(o));
      }
      SBAR();
      sc = sn;
      if (bb == 5) { VMW(0); BAR(); }
      if (bb == 4) BAR();
    }
    if (__builtin_expect(!all_lanes(o[8] < 1.0995116e12f), 0)) {
#pragma clang loop unroll(disable)
      for (int pass = 0; pass < 2; ++pass) {
        o = zero;
#pragma clang loop unroll(disable)
        for (int bb = bstart; bb <= 8; ++bb) { const f32x16 s = MFMA32(ld128(kp + bb * 1024), qf, zero);
          const unsigned fl = bb == 0 ? 0xffffffffu : 0u, al = (bb == 0 || bb == 8) ? 0u : 0xffffffffu;
          const lptr vq = vp8 - (8 - bb) * vstep;
          unsigned p[8];
#pragma unroll
          for (int c = 0; c < 8; ++c) { const unsigned keep = (mk[c] ^ fl) | al;
            p[c] = pk(__builtin_amdgcn_exp2f(s[2 * c] - m), __builtin_amdgcn_exp2f(s[2 * c + 1] - m)) & keep;
            if (pass == 0) { m = (keep & 1u) ? __builtin_fmaxf(m, s[2 * c]) : m; m = (keep >> 31) ? __builtin_fmaxf(m, s[2 * c + 1]) : m; } }
          o = MFMA32(ldtr2(vq, 256), __builtin_bit_cast(bf16x8, u32x4{p[0], p[1], p[2], p[3]}), o);
          o = MFMA32(ldtr2(vq + 512, 256), __builtin_bit_cast(bf16x8, u32x4{p[4], p[5], p[6], p[7]}), o); }
        { auto rr = __builtin_amdgcn_permlane32_swap(f2u(m), f2u(m), false, false); m = __builtin_fmaxf(u2f(rr[0]), u2f(rr[1])); }
      }
    }
    STAMP(6); FT(51);
    const float rl = __builtin_amdgcn_rcpf(o[8]);
    const u32x4 ov = {pk(o[0] * rl, o[1] * rl), pk(o[2] * rl, o[3] * rl), pk(o[4] * rl, o[5] * rl), pk(o[6] * rl, o[7] * rl)};
    st16_sc1(ov, wsr, ovo, WS_HO + (unsigned)(((2 * r + (i >> 1)) * 72 + 56 + hp) * 4 + 2 * (i & 1)) * 1024u);
  }
  {
    const int row = tid >> 3, chunk = tid & 7;
    const size_t go = (size_t)(rh * 64 + row) * 1024 + ct * 128 + chunk * 16;
    u32x4 xv;
    if (ch) {
      unsigned sob = 10 * 131072, da = ldw + 6 * SLOT, db = ldw;
      soa += 32768;
      auto lda = [&]() { item16s(wsr, voffL, soa, da); soa += 16384; da = da == ldw + 8 * SLOT ? ldw + 4 * SLOT : da + SLOT; };
      auto ldb = [&](rsrc_t rb) { item16(rb, vD, sob, db); sob += 131072; db = db == ldw + 3 * SLOT ? ldw : db + SLOT; };
      FT(52); VMW(5); BAR(); FT(53); lda(); lda();
      VMW(9); BAR(); lda(); lda();
#pragma clang loop unroll(disable)
      for (int n = 2; n < 7; ++n) { VMW(12); BAR(); lda(); }
      flags_get(WS_FO + fho);
#pragma clang loop unroll(disable)
      for (int n = 7; n < 10; ++n) { VMW(12); BAR(); lda(); ldb(rw2); }
      VMW(12); BAR(); ldb(rw2); flags_chk(WS_FO + fho); lda();
      const rsrc_t rwo = mkrsrc(wo, D * D * 2);
      sob = 0; vD[1] += 8192; vD[2] -= 8192;
#pragma clang loop unroll(disable)
      for (int n = 11; n < 14; ++n) { VMW(12); BAR(); lda(); ldb(rwo); }
      VMW(12); BAR();
      if (b == 0 && w == 7) { setm0(lds0 + M_CS); pc16s<0>(wsr, voffL, WS_FO); pc16s<1024>(wsr, voffL, WS_FO); }
      ldb(rwo);
      VMW(12); BAR();
      VMW(4); BAR();
      xv = *(const u32x4*)(x + go);
      VMW(0); BAR();
    } else {
      f32x4 dacc[4];
      bf16x8 pa[2][2][4]; s16x4 pb[2][2][2];
      int aA = 4 * SLOT + l * 16; asm volatile("" : "+v"(aA));
      xv = *(const u32x4*)(x + go);
      FT(52);
#pragma unroll
      for (int n = 0; n < 6; ++n) {
        if (n == 2) VMW(0);
        BAR();
        if (n == 0) { FT(53); STAMP(7); }
        if (n == 2 && w == 0) set_flag(tag, wsr, WS_FO + r * 128 + hp * 8);
        if (n == 4 || n == 5) { aA += n == 4 ? 4 * SLOT : -4 * SLOT; asm volatile("" : "+v"(aA)); }
        const lptr ap = smem + aA + (n < 4 ? n * SLOT : 0);
#pragma unroll
        for (int hf = 0; hf < 2; ++hf)
#pragma unroll
          for (int s = 0; s < 2; ++s)
#pragma unroll
            for (int m = 0; m < 4; ++m) {
              const int kk = 4 * n + 2 * hf - 2 + s;
              const bf16x8 t = pa[hf ^ 1][s][m];
              pa[hf][s][m] = ld128(ap + (2 * hf + s) * 4096 + m * 1024);
              SBAR(); if (kk >= 0) MACC(kk == 0, dacc[m], br[kk], t); SBAR(); }
      }
#pragma unroll
      for (int s = 0; s < 2; ++s) { pb[1][s][0] = __builtin_shufflevector(br[22 + s], br[22 + s], 0, 1, 2, 3); pb[1][s][1] = __builtin_shufflevector(br[22 + s], br[22 + s], 4, 5, 6, 7); }
      const int aB = aB0 ^ (32 * rg);
      int ia = SLOT, ib = 0;
#pragma clang loop unroll(disable)
      for (int n = 6; n < 18; ++n) {
        BAR();
        const lptr ap = smem + ia + aA, bp = smem + ib + aB;
#pragma unroll
        for (int hf = 0; hf < 2; ++hf)
#pragma unroll
          for (int s = 0; s < 2; ++s) { const bf16x8 f = cat8(pb[hf ^ 1][s][0], pb[hf ^ 1][s][1]);
#pragma unroll
            for (int m = 0; m < 4; ++m) {
              const bf16x8 t = pa[hf ^ 1][s][m];
              if (m < 2) pb[hf][s][m] = ldtr(bp + (2 * hf + s) * 4096 + m * 2048);
              pa[hf][s][m] = ld128(ap + (2 * hf + s) * 4096 + m * 1024);
              SBAR(); MACC(false, dacc[m], f, t); SBAR(); } }
        ia = ia == 4 * SLOT ? 0 : ia + SLOT; ib = (ib + SLOT) & (4 * SLOT - 1);
      }
#pragma unroll
      for (int s = 0; s < 2; ++s) { const bf16x8 f = cat8(pb[1][s][0], pb[1][s][1]);
#pragma unroll
        for (int m = 0; m < 4; ++m) MACC(false, dacc[m], f, pa[1][s][m]); }
      asm volatile("s_nop 7" : "+v"(dacc[0]), "+v"(dacc[1]), "+v"(dacc[2]), "+v"(dacc[3]));
#pragma unroll
      for (int m = 0; m < 4; ++m) *(LAS f32x4*)(smem + (16 * m + t16) * 256 + (((4 * rg + g4) ^ t16) << 4)) = dacc[m];
    }
    STAMP(8); FT(54);
    const u32x4 bv = *(LAS u32x4*)(smem + M_B2 + chunk * 16);
    BAR();
    const f32x4 a0 = *(LAS f32x4*)(smem + row * 256 + (((2 * chunk) ^ (row & 15)) << 4)), a1 = *(LAS f32x4*)(smem + row * 256 + (((2 * chunk + 1) ^ (row & 15)) << 4));
    u32x4 ov;
    ov[0] = pk(a0[0] + (bflo(xv[0]) + bflo(bv[0])), a0[1] + (bfhi(xv[0]) + bfhi(bv[0])));
    ov[1] = pk(a0[2] + (bflo(xv[1]) + bflo(bv[1])), a0[3] + (bfhi(xv[1]) + bfhi(bv[1])));
    ov[2] = pk(a1[0] + (bflo(xv[2]) + bflo(bv[2])), a1[1] + (bfhi(xv[2]) + bfhi(bv[2])));
    ov[3] = pk(a1[2] + (bflo(xv[3]) + bflo(bv[3])), a1[3] + (bfhi(xv[3]) + bfhi(bv[3])));
    *(u32x4*)(out + go) = ov;
    if (b == 0 && w == 7) {
      const u32x4 f0 = *(LAS u32x4*)(smem + M_CS + l * 16), f1 = *(LAS u32x4*)(smem + M_CS + 1024 + l * 16);
      const unsigned d = (f0[0] ^ tag[0]) | (f0[1] ^ tag[1]) | (f0[2] ^ tag[0]) | (f0[3] ^ tag[1]) | (f1[0] ^ tag[0]) | (f1[1] ^ tag[1]) | (f1[2] ^ tag[0]) | (f1[3] ^ tag[1]);
      if (!all_lanes(d == 0)) {
#pragma clang loop unroll(disable)
        for (int c = 0; c < 8; ++c) poll(ws, WS_FO + (c * 64 + l) * 4, (l & 1) ? tag[1] : tag[0]); }
      set_flag(tag, wsr, WS_EPOCH);
    }
    asm volatile("Lfk_end:");
    STAMP(9); FT(55);
  }
}
}
#if defined(__HIP_DEVICE_COMPILE__)
#pragma clang attribute pop
#endif

extern "C" void kernel_launch(void* const* d_in, const int* in_sizes, int n_in, void* d_out, int out_size, void* d_ws, size_t ws_size, hipStream_t stream) {
  fk::fused<<<256, 512, 0, stream>>>((const char*)d_in[0], (const char*)d_in[1], (const char*)d_in[2], (const char*)d_in[4], (const char*)d_in[5],
                                     (const char*)d_in[6], (const char*)d_in[7], (char*)d_out, (char*)d_ws);
}
```

```cpp
#include <hip/hip_runtime.h>
#include <stdint.h>

#if defined(__HIP_DEVICE_COMPILE__)
#pragma clang attribute push(__attribute__((target("no-packed-fp32-ops"))), apply_to = function)
#endif
namespace fk {
typedef unsigned short u16;
typedef unsigned long long u64;
typedef short bf16x8 __attribute__((ext_vector_type(8)));
typedef short s16x4 __attribute__((ext_vector_type(4)));
typedef float f32x2 __attribute__((ext_vector_type(2)));
typedef float f32x4 __attribute__((ext_vector_type(4)));
typedef float f32x16 __attribute__((ext_vector_type(16)));
typedef unsigned u32x2 __attribute__((ext_vector_type(2)));
typedef unsigned u32x4 __attribute__((ext_vector_type(4)));
typedef __bf16 bf16x2_t __attribute__((ext_vector_type(2)));
#define LAS __attribute__((address_space(3)))
#define GAS __attribute__((address_space(1)))
#define DI __device__ __forceinline__
typedef LAS char* lptr;

constexpr int S = 2048, D = 512, F = 1792;
constexpr unsigned WS_EPOCH = 0;
constexpr unsigned WS_FQKV = 256;
constexpr unsigned WS_FH = WS_FQKV + 16 * 256;
constexpr unsigned WS_FO = WS_FH + 16 * 128;
constexpr unsigned WS_DBG = 16384;
constexpr unsigned WS_QKV = 1u << 20;
constexpr unsigned WS_HO = WS_QKV + 6u * (1u << 20);
constexpr unsigned WS_END = WS_HO + 32u * 72u * 4096u;
constexpr int SLOT = 16384, NSLOT = 9, MISC = NSLOT * SLOT;
constexpr int M_STATS = MISC, M_B1 = MISC + 1024  , M_B2 = MISC + 1536, M_EP = MISC + 1792, M_ONES = MISC + 2048  , M_CNT = MISC + 3072,
              M_TOUCH = MISC + 3328  , M_JUNK = MISC + 3584, M_CS = MISC + 4096  , M_Q = MISC + 8192  ;
constexpr int LDS_BYTES = 163840;

#define MFMA16(a, b, c) __builtin_amdgcn_mfma_f32_16x16x32_bf16((a), (b), (c), 0, 0, 0)
#define MFMA16K16(a, b, c) __builtin_amdgcn_mfma_f32_16x16x16bf16_1k((a), (b), (c), 0, 0, 0)
#define MFMA32(a, b, c) __builtin_amdgcn_mfma_f32_32x32x16_bf16((a), (b), (c), 0, 0, 0)
#define MACC(first, c, a, b) do { if (first) asm volatile("v_mfma_f32_16x16x32_bf16 %0, %1, %2, 0" : "=&v"(c) : "v"(a), "v"(b)); \
                                  else asm volatile("v_mfma_f32_16x16x32_bf16 %0, %1, %2, %0" : "+v"(c) : "v"(a), "v"(b)); } while (0)
#define MEND() asm volatile("s_nop 7" : "+v"(acc[0][0]), "+v"(acc[0][1]), "+v"(acc[0][2]), "+v"(acc[0][3]), "+v"(acc[1][0]), "+v"(acc[1][1]), "+v"(acc[1][2]), "+v"(acc[1][3]))
#define PRIO(n) __builtin_amdgcn_s_setprio(n)
#define HI() do { SBAR(); PRIO(2); SBAR(); } while (0)
#define LO() do { SBAR(); PRIO(0); SBAR(); } while (0)
#define SBAR() __builtin_amdgcn_sched_barrier(0)
#define BAR() asm volatile("s_waitcnt lgkmcnt(0)\n\ts_barrier" ::: "memory")
#define VMW(N) asm volatile("s_waitcnt vmcnt(" #N ")" ::: "memory")
#define RLX_AGENT __ATOMIC_RELAXED, __HIP_MEMORY_SCOPE_AGENT

typedef __amdgpu_buffer_rsrc_t rsrc_t;
DI unsigned rfl(unsigned v) { return __builtin_amdgcn_readfirstlane(v); }
DI rsrc_t mkrsrc(const void* p, unsigned bytes) { return __builtin_amdgcn_make_buffer_rsrc((void*)p, 0, bytes, 0x00020000); }
DI void setm0(unsigned lds_dst) { asm volatile("s_mov_b32 m0, %0\n\ts_nop 0" ::"s"(rfl(lds_dst)) : "memory"); }
template <int OFF> DI void pc16(rsrc_t rs, unsigned voff, unsigned soff) {
  asm volatile("buffer_load_dwordx4 %0, %1, %2 offen offset:%3 lds" ::"v"(voff), "s"(rs), "s"(rfl(soff)), "i"(OFF) : "memory"); }
template <int OFF> DI void pc16s(rsrc_t rs, unsigned voff, unsigned soff) {
  asm volatile("buffer_load_dwordx4 %0, %1, %2 offen offset:%3 sc1 lds" ::"v"(voff), "s"(rs), "s"(rfl(soff)), "i"(OFF) : "memory"); }
DI void item16(rsrc_t rs, const unsigned (&v)[4], unsigned soff, unsigned dst) { setm0(dst); pc16<0>(rs, v[0], soff); pc16<1024>(rs, v[1], soff); pc16<2048>(rs, v[2], soff); pc16<3072>(rs, v[3], soff); }
DI void item16s(rsrc_t rs, unsigned v, unsigned soff, unsigned dst) { setm0(dst); pc16s<0>(rs, v, soff); pc16s<1024>(rs, v, soff); pc16s<2048>(rs, v, soff); pc16s<3072>(rs, v, soff); }
DI void dma4(rsrc_t rs, unsigned voff, unsigned lds_dst) { setm0(lds_dst); asm volatile("buffer_load_dword %0, %1, 0 offen lds" ::"v"(voff), "s"(rs) : "memory"); }
DI void dma4_sc1(rsrc_t rs, unsigned voff, unsigned lds_dst) { setm0(lds_dst); asm volatile("buffer_load_dword %0, %1, 0 offen sc1 lds" ::"v"(voff), "s"(rs) : "memory"); }
DI void st16_sc1(u32x4 v, rsrc_t rs, unsigned voff, unsigned soff) {
  asm volatile("buffer_store_dwordx4 %0, %1, %2, %3 offen sc1\n\ts_nop 1" ::"v"(v), "v"(voff), "s"(rs), "s"(rfl(soff)) : "memory"); }
DI void set_flag(u32x2 tag, rsrc_t rs, unsigned soff) {
  asm volatile("s_mov_b64 exec, 1\n\tbuffer_store_dwordx2 %0, off, %1, %2 sc1\n\ts_mov_b64 exec, -1" ::"v"(tag), "s"(rs), "s"(rfl(soff)) : "memory"); }
DI void count_flag(unsigned cnt_addr, unsigned last, u32x2 tag, rsrc_t rs, unsigned soff) {
  unsigned t;
  asm volatile("s_mov_b64 exec, 1\n\tv_mov_b32 %0, 1\n\tds_add_rtn_u32 %0, %1, %0\n\ts_waitcnt lgkmcnt(0)\n\tv_cmp_eq_u32 vcc, %2, %0\n\ts_cbranch_vccz 1f\n\t"
               "buffer_store_dwordx2 %3, off, %4, %5 sc1\n1:\n\ts_mov_b64 exec, -1"
               : "=&v"(t) : "v"(cnt_addr), "i"(last), "v"(tag), "s"(rs), "s"(rfl(soff)) : "vcc", "memory"); }

DI bf16x8 ld128(lptr p) { return *(LAS bf16x8*)p; }
DI s16x4 ldtr(lptr p) { return __builtin_amdgcn_ds_read_tr16_b64_v4i16((LAS s16x4*)p); }
DI bf16x8 cat8(s16x4 a, s16x4 b) { return __builtin_shufflevector(a, b, 0, 1, 2, 3, 4, 5, 6, 7); }
DI bf16x8 ldtr2(lptr p, int second) { return cat8(ldtr(p), ldtr(p + second)); }
DI unsigned pk(float lo, float hi) { f32x2 f = {lo, hi}; return __builtin_bit_cast(unsigned, __builtin_convertvector(f, bf16x2_t)); }
DI float u2f(unsigned u) { return __builtin_bit_cast(float, u); }
DI unsigned f2u(float f) { return __builtin_bit_cast(unsigned, f); }
DI float bflo(unsigned u) { return u2f(u << 16); }
DI float bfhi(unsigned u) { return u2f(u & 0xffff0000u); }
DI unsigned split2(float v) { const unsigned h = pk(v, 0.f); return pk(v, v - bflo(h)); }
DI lptr lxor(lptr p, unsigned m) { return (lptr)(uintptr_t)((unsigned)(uintptr_t)p ^ m); }
DI bool all_lanes(bool c) { return __builtin_amdgcn_ballot_w64(!c) == 0; }

DI void poll(const char* ws, unsigned off, unsigned want) {
  GAS unsigned* p = (GAS unsigned*)(ws + off);
#pragma clang loop unroll(disable)
  for (unsigned spins = 0; spins < (1u << 16); ++spins) {
    if (all_lanes(__hip_atomic_load(p, RLX_AGENT) == want)) break;
    __builtin_amdgcn_s_sleep(2);
  }
  asm volatile("" ::: "memory");
}

#define FA(i) do { } while (0)
#define FT(i) do { } while (0)
#define STAMP(i) do { } while (0)

__global__ __launch_bounds__(512, 2) void fused(const char* __restrict__ x, const char* __restrict__ wqkv, const char* __restrict__ wo,
                                                const char* __restrict__ w1, const char* __restrict__ b1, const char* __restrict__ w2,
                                                const char* __restrict__ b2, char* __restrict__ out, char* ws) {
  __shared__ __attribute__((aligned(1024))) char smem_[LDS_BYTES];
  const lptr smem = (lptr)smem_;
  const unsigned lds0 = (unsigned)(uintptr_t)smem;
  const int tid = __builtin_amdgcn_workitem_id_x(), l = tid & 63;
  const int w = __builtin_amdgcn_readfirstlane(tid >> 6);
  const int b = __builtin_amdgcn_workgroup_id_x(), xcd = b & 7, jj = b >> 3;
  const int r = (xcd & 1) * 8 + (jj & 7), type = jj >> 3, uq = xcd >> 1;
  const int hp = uq * 4 + type;
  const int rh = xcd * 4 + (jj & 3), ct = jj >> 2;
  const bool q1 = type < 2;
  const int kind0 = 1 + (type >> 1), hq = 2 * uq + (type & 1);
  const int f2 = uq * 7 + (q1 ? type : 2 * type - 1), f3 = uq * 7 + 6;
  const int rg = w & 3, ch = w >> 2, m0 = type & 1;
  STAMP(0); FT(0);
  const unsigned colsw = (unsigned)(((((l & 7) >> 1) ^ ((l >> 4) & 3)) << 5) + ((l & 1) << 4));
  const unsigned rowW = (unsigned)(8 * (l >> 5) + ((l >> 3) & 3) + 32 * rg);
  const rsrc_t r1 = mkrsrc(w1, D * F * 2), wsr = mkrsrc(ws, WS_END);
  const unsigned ldw = lds0 + rg * 4096;
  const int t16 = l & 15, g4 = l >> 4;
  const int aB0 = (4 * g4 + ((l >> 2) & 3)) * 128 + 8 * (l & 3) + ((((g4 & 1) << 1) | ((l >> 3) & 1)) << 5);
  const int aX0 = (32 * rg + 16 * m0 + t16) * 128 + ((g4 ^ (t16 >> 1)) << 4);
  bf16x8 ones = {0x3f80, 0x3f80, 0x3f80, 0x3f80, 0x3f80, 0x3f80, 0x3f80, 0x3f80};
  bf16x8 xf[2][16];
  f32x4 acc[2][4], cs[4];
  s16x4 h[2][4][2], cor[2];
  float rstd_[2], std_[2];
  u32x2 tag;
  unsigned vF[4], vD0, fkv;
  int ac[4];
  auto setup = [&]() {
#pragma unroll
    for (int c = 0; c < 4; ++c) { vF[c] = (rowW + 16 * (c & 1) + 4 * (c >> 1)) * 3584u + colsw - 1024u * c; ac[c] = (aB0 ^ (32 * c)) + rg * 4096;
      asm volatile("" : "+v"(vF[c]), "+v"(ac[c])); }
    vD0 = (unsigned)((32 * rg + (l >> 3)) * 1024 + ct * 128) + colsw;
    int fr = r - 2 + ((l >> 2) & 3); fr = fr < 0 ? 0 : fr > r ? r : fr;
    fkv = WS_FQKV + fr * 256 + (hp >> 1) * 24 + 8 + (l & 3) * 4;
    asm volatile("" : "+v"(ones), "+v"(vD0), "+v"(fkv)); };
  bf16x8 cf[4]; s16x4 fa[4];
  auto cs_rd = [&](int off) {
#pragma unroll
    for (int n = 0; n < 4; ++n) cf[n] = ldtr2(smem + ac[n] + off, 2048);
    SBAR(); };
  auto cs_mm = [&](bool first) {
#pragma unroll
    for (int n = 0; n < 4; ++n) cs[n] = MFMA16(ones, cf[n], first ? f32x4{} : cs[n]);
    SBAR(); };
  auto cs_move = [&](int d) {
#pragma unroll
    for (int n = 0; n < 4; ++n) { ac[n] += d; asm volatile("" : "+v"(ac[n])); } };
  auto put_cs = [&](int buf, unsigned bv) {
    const float c0 = cs[0][0], c1 = cs[1][0], c2 = cs[2][0], c3 = cs[3][0];
    const float lo = (g4 & 1) ? c1 : c0, hi = (g4 & 1) ? c3 : c2;
    const unsigned hl = split2((g4 & 2) ? hi : lo);
    *(LAS u32x2*)(smem + M_CS + buf * 2048 + rg * 512 + l * 8) = u32x2{hl, (hl & 0xffffu) | (bv << 16)}; };
  auto bias16 = [&](int boff, bool on) -> unsigned { const unsigned v = *(LAS u16*)(smem + M_B1 + boff + 2 * l); return on ? v : 0u; };
  auto fix_rd = [&]() {
#pragma unroll
    for (int n = 0; n < 4; ++n) fa[n] = *(LAS s16x4*)(smem + M_CS + ch * 2048 + g4 * 512 + (16 * n + t16) * 8);
    SBAR(); };
  auto fix_mm = [&]() {
#pragma unroll
    for (int n = 0; n < 4; ++n) { acc[0][n] = MFMA16K16(fa[n], cor[0], acc[0][n]); acc[1][n] = MFMA16K16(fa[n], cor[1], acc[1][n]); }
    SBAR(); };
  auto flags_get = [&](unsigned off) { dma4_sc1(wsr, off, lds0 + M_Q + w * 1024); };
  auto flags_chk = [&](unsigned off) { const unsigned want = (l & 1) ? tag[1] : tag[0];
    if (__builtin_expect(!all_lanes(*(LAS unsigned*)(smem + M_Q + w * 1024 + l * 4) == want), 0)) poll(ws, off, want); };
  auto qkv_head = [&](int n, float rs0, float rs1, unsigned lo, unsigned so) {
    const f32x4 y0 = acc[0][n] * rs0, y1 = acc[1][n] * rs1;
    auto s0 = __builtin_amdgcn_permlane16_swap(pk(y0[0], y0[1]), pk(y1[0], y1[1]), false, false);
    auto s1 = __builtin_amdgcn_permlane16_swap(pk(y0[2], y0[3]), pk(y1[2], y1[3]), false, false);
    st16_sc1(u32x4{s0[0], s1[0], s0[1], s1[1]}, wsr, lo, so + n * 65536u); };
  auto gelu8 = [&](f32x4 a, f32x4 c, int m) -> u32x4 {
    const float c1 = -2.f * 0.7978845608028654f * 1.4426950408889634f, k1 = c1 * rstd_[m], k2 = (c1 * 0.044715f) * rstd_[m] * rstd_[m] * rstd_[m], sd = std_[m];
    float y[8];
#pragma unroll
    for (int e = 0; e < 8; ++e) { const float v = e < 4 ? a[e & 3] : c[e & 3];
      const float ex = __builtin_amdgcn_exp2f(v * __builtin_fmaf(v * v, k2, k1));
      y[e] = v * __builtin_amdgcn_rcpf(__builtin_fmaf(ex, sd, sd)); }
    return u32x4{pk(y[0], y[1]), pk(y[2], y[3]), pk(y[4], y[5]), pk(y[6], y[7])}; };
  const unsigned sF2 = (unsigned)f2 * 128u, sF3 = (unsigned)f3 * 128u;
  constexpr unsigned QF = 128 * 3584;

  if (ch) {
    __builtin_amdgcn_s_setprio(2);
    unsigned vQ[4], vX[4];
#pragma unroll
    for (int c = 0; c < 4; ++c) { vX[c] = (unsigned)((32 * rg + (l >> 3)) * 1024 + c * 7168 + (((l & 7) ^ ((4 * (c & 1) + (l >> 4)) & 7)) << 4));
      vQ[c] = (rowW + 16 * (c & 1) + 4 * (c >> 1)) * 3072u + colsw - 1024u * c; }
    const rsrc_t rx = mkrsrc(x, S * D * 2), rq = mkrsrc(wqkv, D * 3 * D * 2);
    const unsigned c0 = (unsigned)(kind0 * 1024 + hq * 128), xo = (unsigned)r * 131072u;
    auto trip = [&](int u, int i) { const int s0 = (3 * u) % NSLOT;
      if (i < 2) item16(rx, vX, xo + u * 256 + i * 128, ldw + (s0 + i) * SLOT); else item16(rq, vQ, c0 + u * (128 * 3072), ldw + (s0 + 2) * SLOT); };
    trip(0, 0); trip(0, 2); trip(0, 1); trip(1, 0); trip(1, 2); trip(1, 1);
    setup();
    const rsrc_t rs1 = mkrsrc(q1 ? wqkv : w1, q1 ? D * 3 * D * 2 : D * F * 2);
    const unsigned c1 = (unsigned)(q1 ? hq : f2 - 1) * 128u, st1 = q1 ? 128 * 3072 : QF;
    f32x4 sm[2] = {}, sq[2] = {};
    cs[0] = cs[1] = cs[2] = cs[3] = f32x4{};
    int xb4[4];
#pragma unroll
    for (int n = 0; n < 4; ++n) { xb4[n] = aX0 ^ (64 * (n >> 1)) ^ (2048 * (n & 1)); asm volatile("" : "+v"(xb4[n])); }
#pragma unroll
    for (int u = 0; u < 4; ++u) {
      if (u > 0) { const int d = u == 3 ? -6 * SLOT : 3 * SLOT; cs_move(d);
#pragma unroll
        for (int n = 0; n < 4; ++n) { xb4[n] += d; asm volatile("" : "+v"(xb4[n])); } }
      if (u == 2) {
#pragma unroll
        for (int c = 0; c < 4; ++c) vQ[c] = q1 ? vQ[c] : vF[c]; }
      FT(1 + 3 * u); VMW(12); BAR(); FT(3 + 3 * u);
      if (u == 3) {
#pragma unroll
        for (int c = 0; c < 4; ++c) { xf[0][12 + c] = ld128(smem + xb4[2 * (c & 1)] + (c >> 1) * SLOT); xf[1][12 + c] = ld128(smem + xb4[2 * (c & 1) + 1] + (c >> 1) * SLOT); }
        SBAR(); item16(rs1, vQ, c1 + 3 * st1, ldw + 6 * SLOT); }
#pragma unroll
      for (int c = 0; c < 4; ++c) {
        const int k = 4 * u + c;
        if (u < 3) { xf[0][k] = ld128(smem + xb4[2 * (c & 1)] + (c >> 1) * SLOT); xf[1][k] = ld128(smem + xb4[2 * (c & 1) + 1] + (c >> 1) * SLOT); }
        cf[c] = ldtr2(smem + 2 * SLOT + ac[c], 2048);
        SBAR();
        if (c < 3) {
          if (u < 2) trip(u + 2, c);
          if (u == 2) item16(rs1, vQ, c1 + c * st1, ldw + (3 + c) * SLOT);
        }
        sm[0] = MFMA16(ones, xf[0][k], sm[0]); sq[0] = MFMA16(xf[0][k], xf[0][k], sq[0]);
        sm[1] = MFMA16(ones, xf[1][k], sm[1]); sq[1] = MFMA16(xf[1][k], xf[1][k], sq[1]);
        if (u < 3) cs[c] = MFMA16(ones, cf[c], cs[c]);
        SBAR();
      }
      if (u == 3) {
#pragma unroll
        for (int c = 0; c < 4; ++c) cs[c] = MFMA16(ones, cf[c], cs[c]); }
    }
    { const bool e0 = l & 1, e1 = l & 2;
      const float d0 = e1 ? (e0 ? sq[0][3] : sq[0][2]) : (e0 ? sq[0][1] : sq[0][0]), d1 = e1 ? (e0 ? sq[1][3] : sq[1][2]) : (e0 ? sq[1][1] : sq[1][0]);
      const int src = (t16 + 16 * (t16 >> 2)) * 4;
      const float ss[2] = {u2f(__builtin_amdgcn_ds_bpermute(src, f2u(d0))), u2f(__builtin_amdgcn_ds_bpermute(src, f2u(d1)))};
      const u32x2 ep = *(LAS u32x2*)(smem + M_EP);
      const u64 t = (((u64)ep[1] << 32) | ep[0]) + 1; tag = u32x2{(unsigned)t, (unsigned)(t >> 32)};
#pragma unroll
      for (int m = 0; m < 2; ++m) {
        const float mean = sm[m][0] * (1.f / D), ve = __builtin_fmaxf(__builtin_fmaf(ss[m], 1.f / D, -mean * mean), 0.f) + 1e-5f;
        rstd_[m] = __builtin_amdgcn_rsqf(ve); std_[m] = ve * rstd_[m];
        const unsigned mm = split2(-mean), sd = split2(std_[m]);
        cor[m] = __builtin_bit_cast(s16x4, u32x2{(mm & 0xffffu) | (mm << 16), (mm >> 16) | (g4 == 0 ? sd << 16 : sd & 0xffff0000u)}); }
      const u32x2 c0_ = __builtin_bit_cast(u32x2, cor[0]), c1_ = __builtin_bit_cast(u32x2, cor[1]);
      *(LAS u32x4*)(smem + M_Q + rg * 1024 + l * 16) = u32x4{c0_[0], c0_[1], c1_[0], c1_[1]};
      *(LAS f32x4*)(smem + M_Q + 4096 + rg * 1024 + l * 16) = f32x4{rstd_[0], rstd_[1], std_[0], std_[1]};
      *(LAS u32x2*)(smem + M_EP + 8) = tag; }
    put_cs(0, 0);
    __builtin_amdgcn_s_setprio(0);
    FT(13); VMW(8);
  } else {
    { u64 pc; unsigned csz; asm volatile("s_getpc_b64 %0\nLfk_pc:\n\ts_mov_b32 %1, Lfk_end-Lfk_pc" : "=s"(pc), "=s"(csz));
      if (w == 0) dma4(mkrsrc(x, S * D * 2), (unsigned)((r * 16 + hp) * 64 + l) * 128u, lds0 + M_TOUCH);
      if (w == 2) dma4(mkrsrc(wqkv, D * 3 * D * 2), (unsigned)(b * 48 + (l < 48 ? l : 0)) * 128u, lds0 + M_TOUCH);
      if (w == 3) dma4(mkrsrc((const void*)pc, csz), (unsigned)(jj * 8 + (l & 7)) * 128u, lds0 + M_TOUCH); }
    if (w == 0) dma4_sc1(wsr, (unsigned)(l & 1) * 4u, lds0 + M_EP);
    if (w == 1) { const rsrc_t rb = mkrsrc(b1, F * 2); dma4(rb, (unsigned)(l * 4 + (f2 - 1) * 128), lds0 + M_B1); dma4(rb, (unsigned)(l * 4 + f3 * 128), lds0 + M_B1 + 256); }
    if (w == 2) dma4(mkrsrc(b2, D * 2), (unsigned)(l * 4 + ct * 128), lds0 + M_B2);
    if (w == 3) { const unsigned o1 = 4 * r - 8 + t16 >= 0 ? 0x3f803f80u : 0u; *(LAS u32x2*)(smem + M_ONES + g4 * 256 + t16 * 8) = u32x2{o1, o1}; *(LAS unsigned*)(smem + M_CNT + l * 4) = 0u; }
    setup();
    int wb4[4], xb4[4];
#pragma unroll
    for (int n = 0; n < 4; ++n) { wb4[n] = (aB0 ^ (32 * n)) + 2 * SLOT; xb4[n] = aX0 ^ (64 * (n >> 1)) ^ (2048 * (n & 1)); asm volatile("" : "+v"(wb4[n]), "+v"(xb4[n])); }
    FT(2); BAR(); FT(3);
    xf[0][0] = ld128(smem + xb4[0]); xf[1][0] = ld128(smem + xb4[1]);
#pragma unroll
    for (int n = 0; n < 4; ++n) { h[0][n][0] = ldtr(smem + wb4[n]); h[0][n][1] = ldtr(smem + wb4[n] + 2048); }
#pragma unroll
    for (int k = 0; k < 16; ++k) {
      const int kn = k + 1, c = k & 1;
      if ((k & 3) == 3 && k < 15) {
#pragma unroll
        for (int n = 0; n < 4; ++n) { const int d = k == 11 ? -6 * SLOT : 3 * SLOT; wb4[n] += d; xb4[n] += d; asm volatile("" : "+v"(wb4[n]), "+v"(xb4[n])); }
        if (k == 11) VMW(0);
        if (k == 3 && w == 1) dma4(r1, (unsigned)(b * 56 + (l < 56 ? l : 0)) * 128u, lds0 + M_TOUCH);
        if (k == 11 && w >= 2) { const int nl = w == 2 ? 56 : 16; dma4(mkrsrc(w == 2 ? w2 : wo, nl * 32768), (unsigned)(b * nl + (l < nl ? l : 0)) * 128u, lds0 + M_TOUCH); }
        FT(2 + 3 * (kn >> 2)); BAR(); FT(3 + 3 * (kn >> 2)); }
#pragma unroll
      for (int n = 0; n < 4; ++n) {
        const bf16x8 bf = cat8(h[c][n][0], h[c][n][1]);
        if (k < 15) { h[c ^ 1][n][0] = ldtr(smem + wb4[n] + (kn & 3) * 4096); if (n < 2) xf[n][kn] = ld128(smem + xb4[2 * (kn & 1) + n] + ((kn & 3) >> 1) * SLOT); }
        SBAR(); MACC(k == 0, acc[0][n], bf, xf[0][k]); SBAR();
        if (k < 15) h[c ^ 1][n][1] = ldtr(smem + wb4[n] + (kn & 3) * 4096 + 2048);
        SBAR(); MACC(k == 0, acc[1][n], bf, xf[1][k]); SBAR();
      }
    }
    MEND();
  }
  const int tok = (l & 31) ^ (16 * m0);
  const unsigned qso = WS_QKV + (unsigned)(hq * 256 + 4 * r + rg) * 1024u;
  unsigned lo0 = kind0 == 2 ? tok * 32 + (l >> 5) * 16 : (l >> 5) * 512 + tok * 16, so0 = qso + kind0 * (32u * 65536u);
  if (ch == 0) { cs_move(3 * SLOT); asm volatile("" : "+v"(lo0), "+s"(so0)); PRIO(2); }
  FT(14); BAR(); FT(15);
  STAMP(1);

  if (ch == 0) {
    { const u32x4 c4 = *(LAS u32x4*)(smem + M_Q + rg * 1024 + l * 16); const f32x4 s4 = *(LAS f32x4*)(smem + M_Q + 4096 + rg * 1024 + l * 16);
      cor[0] = __builtin_bit_cast(s16x4, u32x2{c4[0], c4[1]}); cor[1] = __builtin_bit_cast(s16x4, u32x2{c4[2], c4[3]});
      rstd_[0] = s4[0]; rstd_[1] = s4[1]; std_[0] = s4[2]; std_[1] = s4[3]; tag = *(LAS u32x2*)(smem + M_EP + 8); }
    fix_rd(); cs_rd(0);
    item16(r1, vF, sF2, ldw + 7 * SLOT);
    FT(16); fix_mm(); cs_mm(true); cs_rd(SLOT);
    qkv_head(0, rstd_[0], rstd_[1], lo0, so0); SBAR();
    item16(r1, vF, sF2 + QF, ldw + 8 * SLOT);
    cs_mm(false);
    qkv_head(1, rstd_[0], rstd_[1], lo0, so0); SBAR();
    item16(r1, vF, sF2 + 2 * QF, ldw);
    qkv_head(2, rstd_[0], rstd_[1], lo0, so0); SBAR();
    FT(20); BAR(); FT(21);
    cs_rd(2 * SLOT);
    qkv_head(3, rstd_[0], rstd_[1], lo0, so0); SBAR();
    cs_mm(false); cs_rd(3 * SLOT);
    const unsigned bv = bias16(0, !q1 && rg < 2);
    item16(r1, vF, sF2 + 3 * QF, ldw + SLOT);
    cs_mm(false);
    item16(r1, vF, sF3, ldw + 2 * SLOT);
    put_cs(1, bv); SBAR();
    item16(r1, vF, sF3 + QF, ldw + 3 * SLOT);
    item16(r1, vF, sF3 + 2 * QF, ldw + 4 * SLOT);
    VMW(16);
    FT(24); BAR(); FT(25);
  }
  {
    const int sa = ch ? 3 : 7, sb = ch ? 5 : 0;
    int a4[4];
    PRIO(1);
#pragma unroll
    for (int n = 0; n < 4; ++n) { a4[n] = (aB0 ^ (32 * n)) + sa * SLOT; h[0][n][0] = ldtr(smem + a4[n]); h[0][n][1] = ldtr(smem + a4[n] + 2048); }
#pragma unroll
    for (int k = 0; k < 16; ++k) {
      const int kn = k + 1, c = k & 1;
      if (k == 7) { FT(26); VMW(0); BAR(); FT(27);
#pragma unroll
        for (int n = 0; n < 4; ++n) a4[n] += (sb - sa) * SLOT; }
#pragma unroll
      for (int n = 0; n < 4; ++n) {
        const bf16x8 bf = cat8(h[c][n][0], h[c][n][1]);
        if (k < 15) h[c ^ 1][n][0] = ldtr(smem + a4[n] + (kn & 7) * 4096);
        SBAR(); MACC(k == 0, acc[0][n], bf, xf[0][k]); SBAR();
        if (k < 15) h[c ^ 1][n][1] = ldtr(smem + a4[n] + (kn & 7) * 4096 + 2048);
        SBAR(); MACC(k == 0, acc[1][n], bf, xf[1][k]); SBAR();
      }
    }
    MEND();
    PRIO(2);
  }
  const unsigned voffL = (unsigned)l * 16u;
  const rsrc_t rw2 = mkrsrc(w2, F * D * 2);
  const unsigned qsrc = WS_QKV + 2 * hp * 65536;
  const unsigned fq = WS_FQKV + r * 256 + (hp >> 1) * 24 + (l & 1) * 4;
  const unsigned hbase = WS_HO + (unsigned)((2 * r + (rg >> 1)) * 288 + 2 * (rg & 1) + m0) * 1024u;
  if (ch) cs_move(7 * SLOT);
  STAMP(2);
  FT(28); BAR(); FT(29);
  {
    const bool isq = ch && q1;
    const unsigned hs = hbase + (unsigned)(f2 - ch) * 8192u;
    const unsigned lo = (l >> 5) * 512 + tok * 16;
    const float sc = 0.25f * 1.4426950408889634f;
    fix_rd();
    if (ch) { cs_rd(0);
      if (w == 4) set_flag(tag, wsr, WS_FQKV + r * 256 + (hq * 3 + kind0) * 8);
      item16(r1, vF, sF3 + 3 * QF, ldw + 5 * SLOT); }
    else flags_get(fq);
    fix_mm();
    if (ch) { cs_mm(true); cs_rd(SLOT); }
    FT(30);
    if (isq) { const float rs0 = rstd_[0] * sc, rs1 = rstd_[1] * sc;
#pragma unroll
      for (int n = 0; n < 4; ++n) qkv_head(n, rs0, rs1, lo, qso); }
    else { if (ch) LO(); st16_sc1(gelu8(acc[0][0], acc[0][1], 0), wsr, voffL, hs); SBAR(); st16_sc1(gelu8(acc[1][0], acc[1][1], 1), wsr, voffL, hs ^ 1024u); HI(); }
    SBAR(); FT(31);
    if (ch) {
      flags_get(fkv);
      cs_mm(false); cs_move(-7 * SLOT);
      if (q1) VMW(1);
      FT(36); BAR(); FT(37);
      cs_rd(0);
      const unsigned bv = bias16(128, rg < 2);
      if (q1 && w == 4) { set_flag(tag, wsr, WS_FQKV + r * 256 + hq * 24); VMW(1); } else VMW(0);
      flags_chk(fkv);
#pragma unroll
      for (int j = 0; j < 3; ++j) { const int rr = r - 2 + j;
        if (j == 0) { cs_mm(false); cs_rd(SLOT); }
        item16s(wsr, voffL, rr < 0 ? WS_END : qsrc + ((rg & 1) + 32 + 32 * (rg >> 1)) * 65536 + rr * 4096, lds0 + 6 * SLOT + rg * 12288 + j * 4096);
        if (j == 0) cs_mm(false);
        if (j == 1) put_cs(0, bv);
        SBAR(); }
    } else { VMW(2); flags_chk(fq); setm0(lds0 + M_Q + w * 1024); pc16s<0>(wsr, voffL, qsrc + (4 * r + rg) * 1024); }
    FT(39);
    if (!isq) { if (ch) LO(); st16_sc1(gelu8(acc[0][2], acc[0][3], 0), wsr, voffL, hs + 4096); SBAR(); st16_sc1(gelu8(acc[1][2], acc[1][3], 1), wsr, voffL, (hs + 4096) ^ 1024u); }
    if (ch) { FT(44); BAR(); FT(45); }
  }
  unsigned vD[4] = {vD0, vD0 + 7168, vD0 + 2 * 7168, vD0 + 3 * 7168};
  if (ch) {
    flags_get(fq);
    PRIO(1);
    FT(46);
    const lptr pa = smem + 2 * SLOT + (aB0 ^ (64 * (type >> 1))), pb = lxor(pa, 32);
    const lptr bp = smem + M_B1 + 256 + 64 * (type >> 1) + 2 * t16;
    unsigned ba = *(LAS u16*)bp, bb = *(LAS u16*)(bp + 32);
    f32x4 qa, qb, ca, cb;
    s16x4 g[3][4];
#pragma unroll
    for (int k = 0; k < 2; ++k) { g[k][0] = ldtr(pa + k * 4096); g[k][1] = ldtr(pa + k * 4096 + 2048); g[k][2] = ldtr(pb + k * 4096); g[k][3] = ldtr(pb + k * 4096 + 2048); }
#pragma unroll
    for (int k = 0; k < 16; ++k) {
      const bf16x8 ga = cat8(g[k % 3][0], g[k % 3][1]), gb_ = cat8(g[k % 3][2], g[k % 3][3]);
      const int kn = k + 2, c = kn % 3;
      if (k == 1 || k == 5) item16(rw2, vD, (k >> 2) * 131072, ldw + (k >> 2) * SLOT);
      if (k == 10) { VMW(8); flags_chk(fq); setm0(lds0 + M_Q + w * 1024); pc16s<0>(wsr, voffL, qsrc + 65536 + (4 * r + rg) * 1024); }
      if (kn < 16) g[c][0] = ldtr(pa + kn * 4096);
      SBAR(); MACC(k == 0, qa, ga, xf[0][k]); SBAR();
      if (kn < 16) g[c][1] = ldtr(pa + kn * 4096 + 2048);
      SBAR(); MACC(k == 0, qb, gb_, xf[0][k]); SBAR();
      if (kn < 16) g[c][2] = ldtr(pb + kn * 4096);
      SBAR(); MACC(k == 0, ca, ones, ga); SBAR();
      if (kn < 16) g[c][3] = ldtr(pb + kn * 4096 + 2048);
      SBAR(); MACC(k == 0, cb, ones, gb_); SBAR();
    }
    asm volatile("s_nop 7" : "+v"(qa), "+v"(qb), "+v"(ca), "+v"(cb));
    FT(47);
    ba = l < 32 ? ba << 16 : 0; bb = l < 32 ? bb << 16 : 0;
    const unsigned ha = split2(l < 16 ? ca[0] : 0.f), hb = split2(l < 16 ? cb[0] : 0.f);
    qa = MFMA16K16(__builtin_bit_cast(s16x4, u32x2{ha, (ha & 0xffffu) | ba}), cor[0], qa);
    qb = MFMA16K16(__builtin_bit_cast(s16x4, u32x2{hb, (hb & 0xffffu) | bb}), cor[0], qb);
    st16_sc1(gelu8(qa, qb, 0), wsr, voffL, hbase + (unsigned)f3 * 8192u + (type >> 1) * 4096);
    { const int z = (l & 31) - 4 * (l >> 5) - 8 * rg;
      *(LAS u32x2*)(smem + M_CS + 2048 + l * 32 + rg * 8) = u32x2{1 <= z ? 0xffffffffu : 0 <= z ? 0xffffu : 0u, 3 <= z ? 0xffffffffu : 2 <= z ? 0xffffu : 0u}; }
    FT(48); VMW(1);
  } else VMW(2);
  const unsigned fho = (unsigned)((rh >> 1) * 128 + (l & 31) * 4);
  unsigned soa = WS_HO + (unsigned)(rh * 72 + rg) * 4096u;
  bf16x8 br[24];
  {
    const int e = ch, i = rg, hh = l >> 5, iq = l & 31;
    const lptr kp = smem + 6 * SLOT + e * 12288 + i * 1024 + l * 16;
    int bstart = 8 - 4 * r - i; bstart = bstart < 0 ? 0 : bstart;
    const bool onesl = (l & 16) != 0;
    const lptr vp8 = onesl ? smem + M_ONES + 8 * (i + 8) : smem + 6 * SLOT + 24576 + e * 12288 + (i + 8) * 1024 + (4 * hh + (t16 >> 2)) * 32 + (l & 3) * 8;
    const int vstep = onesl ? 8 : 1024;
    const unsigned ovo = (unsigned)((iq >> 4) * 1024 + ((iq & 15) + 16 * (2 * e + hh)) * 16);
    STAMP(3);
    FT(49); BAR(); FT(50);
    __builtin_amdgcn_s_setprio(0);
    STAMP(5);
    const bf16x8 qf = ld128(smem + M_Q + w * 1024 + l * 16), k8 = ld128(kp + 8 * 1024);
    bf16x8 kn = ld128(kp + 7 * 1024);
    const u32x4 mka = *(LAS u32x4*)(smem + M_CS + 2048 + l * 32), mkb = *(LAS u32x4*)(smem + M_CS + 2048 + l * 32 + 16);
    const unsigned mk[8] = {mka[0], mka[1], mka[2], mka[3], mkb[0], mkb[1], mkb[2], mkb[3]};
    int ab0 = aB0 ^ (32 * rg), ab1 = ab0 + 2 * SLOT; asm volatile("" : "+v"(ab0), "+v"(ab1));
    const f32x16 zero = {};
    float m;
    { const f32x16 s = MFMA32(k8, qf, zero); m = u2f(__builtin_amdgcn_permlane32_swap(f2u(s[0]), f2u(s[0]), false, false)[0]); }
    f32x16 o, nm;
#pragma unroll
    for (int c = 0; c < 16; ++c) nm[c] = -m;
    lptr vp = vp8;
    f32x16 sc = MFMA32(k8, qf, nm);
    FA(31);
#pragma unroll
    for (int bb = 8; bb >= 0; --bb) {
      const bf16x8 v0 = ldtr2(vp, 256), v1 = ldtr2(vp + 512, 256);
      f32x16 sn;
      if (bb > 0) sn = MFMA32(kn, qf, nm);
      if (bb > 1) kn = ld128(kp + (bb - 2) * 1024);
      vp -= vstep;
      const bool ld = bb >= 6 || bb == 3 || bb == 2, lda_ = ch && bb <= 3;
      const int it = bb >= 7 ? 10 - bb + 2 * ch : bb == 6 ? 6 + ch : 11 - bb + 2 * ch, sl = bb >= 7 ? it : it - 6;
      const unsigned so = lda_ ? soa + (3 - bb) * 16384 : (unsigned)it * 131072u;
      if (bb == 6 && w == 4) set_flag(tag, wsr, WS_FH + r * 128 + hp * 8);
      if (bb == 4 && ch) flags_get(WS_FH + fho);
      if (bb == 3 && ch) { VMW(0); flags_chk(WS_FH + fho); }
      if (ld) setm0(ldw + sl * SLOT);
      unsigned p[8];
#define ATT_Q(q) do { if (ld) { if (bb <= 3 && lda_) pc16s<1024 * q>(wsr, voffL, so); else pc16<1024 * q>(rw2, vD[q], so); } \
        if (bb >= 7) br[4 * (8 - bb) + q] = ldtr2(smem + ab0 + (4 * (8 - bb) + q) * 4096, 2048); \
        if (bb == 4 && ch == 0) { _Pragma("unroll") for (int k = 4 * q; k < 4 * q + 4; ++k) br[8 + k] = ldtr2(smem + ab1 + k * 4096, 2048); } SBAR(); \
        { _Pragma("unroll") for (int c = 2 * q; c < 2 * q + 2; ++c) { p[c] = pk(__builtin_amdgcn_exp2f(sc[2 * c]), __builtin_amdgcn_exp2f(sc[2 * c + 1])); \
            if (bb == 8) p[c] &= mk[c]; if (bb == 0) p[c] &= ~mk[c]; } \
          if (q == 1) o = MFMA32(v0, __builtin_bit_cast(bf16x8, u32x4{p[0], p[1], p[2], p[3]}), bb == 8 ? zero : o); \
          if (q == 3) o = MFMA32(v1, __builtin_bit_cast(bf16x8, u32x4{p[4], p[5], p[6], p[7]}), o); } SBAR(); } while (0)
      ATT_Q(0); ATT_Q(1); ATT_Q(2); ATT_Q(3);
      SBAR();
      sc = sn;
      FA(2 * (8 - bb) + 1);
      if (bb == 7) { VMW(8); BAR(); }
      if (bb == 5) { VMW(4); FA(30); BAR(); }
      if (bb == 4) BAR();
      FA(2 * (8 - bb) + 2);
    }
    if (__builtin_expect(!all_lanes(o[8] < 1.0995116e12f), 0)) {
#pragma clang loop unroll(disable)
      for (int pass = 0; pass < 2; ++pass) {
        o = zero;
#pragma clang loop unroll(disable)
        for (int bb = bstart; bb <= 8; ++bb) { const f32x16 s = MFMA32(ld128(kp + bb * 1024), qf, zero);
          const unsigned fl = bb == 0 ? 0xffffffffu : 0u, al = (bb == 0 || bb == 8) ? 0u : 0xffffffffu;
          const lptr vq = vp8 - (8 - bb) * vstep;
          unsigned p[8];
#pragma unroll
          for (int c = 0; c < 8; ++c) { const unsigned keep = (mk[c] ^ fl) | al;
            p[c] = pk(__builtin_amdgcn_exp2f(s[2 * c] - m), __builtin_amdgcn_exp2f(s[2 * c + 1] - m)) & keep;
            if (pass == 0) { m = (keep & 1u) ? __builtin_fmaxf(m, s[2 * c]) : m; m = (keep >> 31) ? __builtin_fmaxf(m, s[2 * c + 1]) : m; } }
          o = MFMA32(ldtr2(vq, 256), __builtin_bit_cast(bf16x8, u32x4{p[0], p[1], p[2], p[3]}), o);
          o = MFMA32(ldtr2(vq + 512, 256), __builtin_bit_cast(bf16x8, u32x4{p[4], p[5], p[6], p[7]}), o); }
        { auto rr = __builtin_amdgcn_permlane32_swap(f2u(m), f2u(m), false, false); m = __builtin_fmaxf(u2f(rr[0]), u2f(rr[1])); }
      }
    }
    STAMP(6); FT(51);
    const float rl = __builtin_amdgcn_rcpf(o[8]);
    const u32x4 ov = {pk(o[0] * rl, o[1] * rl), pk(o[2] * rl, o[3] * rl), pk(o[4] * rl, o[5] * rl), pk(o[6] * rl, o[7] * rl)};
    st16_sc1(ov, wsr, ovo, WS_HO + (unsigned)(((2 * r + (i >> 1)) * 72 + 56 + hp) * 4 + 2 * (i & 1)) * 1024u);
  }
  {
    const int row = tid >> 3, chunk = tid & 7;
    const size_t go = (size_t)(rh * 64 + row) * 1024 + ct * 128 + chunk * 16;
    u32x4 xv;
    if (ch) {
      unsigned sob = 10 * 131072, da = ldw + 6 * SLOT, db = ldw;
      soa += 32768;
      auto lda = [&]() { item16s(wsr, voffL, soa, da); soa += 16384; da = da == ldw + 8 * SLOT ? ldw + 4 * SLOT : da + SLOT; };
      auto ldb = [&](rsrc_t rb) { item16(rb, vD, sob, db); sob += 131072; db = db == ldw + 3 * SLOT ? ldw : db + SLOT; };
      FT(52); VMW(5); BAR(); FT(53); lda(); lda();
      VMW(9); BAR(); lda(); lda();
#pragma clang loop unroll(disable)
      for (int n = 2; n < 7; ++n) { VMW(12); BAR(); lda(); }
      flags_get(WS_FO + fho);
#pragma clang loop unroll(disable)
      for (int n = 7; n < 10; ++n) { VMW(12); BAR(); lda(); ldb(rw2); }
      VMW(12); BAR(); ldb(rw2); flags_chk(WS_FO + fho); lda();
      const rsrc_t rwo = mkrsrc(wo, D * D * 2);
      sob = 0; vD[1] += 8192; vD[2] -= 8192;
#pragma clang loop unroll(disable)
      for (int n = 11; n < 14; ++n) { VMW(12); BAR(); lda(); ldb(rwo); }
      VMW(12); BAR();
      if (b == 0 && w == 7) { setm0(lds0 + M_CS); pc16s<0>(wsr, voffL, WS_FO); pc16s<1024>(wsr, voffL, WS_FO); }
      ldb(rwo);
      VMW(12); BAR();
      VMW(4); BAR();
      xv = *(const u32x4*)(x + go);
      VMW(0); BAR();
    } else {
      f32x4 dacc[4];
      bf16x8 pa[2][2][4]; s16x4 pb[2][2][2];
      int aA = 4 * SLOT + l * 16; asm volatile("" : "+v"(aA));
      FT(52);
#pragma unroll
      for (int n = 0; n < 6; ++n) {
        if (n == 2) VMW(0);
        BAR();
        if (n == 0) { FT(53); STAMP(7); }
        if (n == 2 && w == 0) set_flag(tag, wsr, WS_FO + r * 128 + hp * 8);
        if (n == 4 || n == 5) { aA += n == 4 ? 4 * SLOT : -4 * SLOT; asm volatile("" : "+v"(aA)); }
        const lptr ap = smem + aA + (n < 4 ? n * SLOT : 0);
#pragma unroll
        for (int hf = 0; hf < 2; ++hf)
#pragma unroll
          for (int s = 0; s < 2; ++s)
#pragma unroll
            for (int m = 0; m < 4; ++m) {
              const int kk = 4 * n + 2 * hf - 2 + s;
              const bf16x8 t = pa[hf ^ 1][s][m];
              pa[hf][s][m] = ld128(ap + (2 * hf + s) * 4096 + m * 1024);
              SBAR(); if (kk >= 0) MACC(kk == 0, dacc[m], br[kk], t); SBAR(); }
      }
#pragma unroll
      for (int s = 0; s < 2; ++s) { pb[1][s][0] = __builtin_shufflevector(br[22 + s], br[22 + s], 0, 1, 2, 3); pb[1][s][1] = __builtin_shufflevector(br[22 + s], br[22 + s], 4, 5, 6, 7); }
      const int aB = aB0 ^ (32 * rg);
      int ia = SLOT, ib = 0;
      xv = *(const u32x4*)(x + go);
#pragma clang loop unroll(disable)
      for (int n = 6; n < 18; ++n) {
        BAR();
        const lptr ap = smem + ia + aA, bp = smem + ib + aB;
#pragma unroll
        for (int hf = 0; hf < 2; ++hf)
#pragma unroll
          for (int s = 0; s < 2; ++s) { const bf16x8 f = cat8(pb[hf ^ 1][s][0], pb[hf ^ 1][s][1]);
#pragma unroll
            for (int m = 0; m < 4; ++m) {
              const bf16x8 t = pa[hf ^ 1][s][m];
              if (m < 2) pb[hf][s][m] = ldtr(bp + (2 * hf + s) * 4096 + m * 2048);
              pa[hf][s][m] = ld128(ap + (2 * hf + s) * 4096 + m * 1024);
              SBAR(); MACC(false, dacc[m], f, t); SBAR(); } }
        ia = ia == 4 * SLOT ? 0 : ia + SLOT; ib = (ib + SLOT) & (4 * SLOT - 1);
      }
#pragma unroll
      for (int s = 0; s < 2; ++s) { const bf16x8 f = cat8(pb[1][s][0], pb[1][s][1]);
#pragma unroll
        for (int m = 0; m < 4; ++m) MACC(false, dacc[m], f, pa[1][s][m]); }
      asm volatile("s_nop 7" : "+v"(dacc[0]), "+v"(dacc[1]), "+v"(dacc[2]), "+v"(dacc[3]));
#pragma unroll
      for (int m = 0; m < 4; ++m) *(LAS f32x4*)(smem + (16 * m + t16) * 256 + (((4 * rg + g4) ^ t16) << 4)) = dacc[m];
    }
    STAMP(8); FT(54);
    const u32x4 bv = *(LAS u32x4*)(smem + M_B2 + chunk * 16);
    BAR();
    const f32x4 a0 = *(LAS f32x4*)(smem + row * 256 + (((2 * chunk) ^ (row & 15)) << 4)), a1 = *(LAS f32x4*)(smem + row * 256 + (((2 * chunk + 1) ^ (row & 15)) << 4));
    u32x4 ov;
    ov[0] = pk(a0[0] + (bflo(xv[0]) + bflo(bv[0])), a0[1] + (bfhi(xv[0]) + bfhi(bv[0])));
    ov[1] = pk(a0[2] + (bflo(xv[1]) + bflo(bv[1])), a0[3] + (bfhi(xv[1]) + bfhi(bv[1])));
    ov[2] = pk(a1[0] + (bflo(xv[2]) + bflo(bv[2])), a1[1] + (bfhi(xv[2]) + bfhi(bv[2])));
    ov[3] = pk(a1[2] + (bflo(xv[3]) + bflo(bv[3])), a1[3] + (bfhi(xv[3]) + bfhi(bv[3])));
    *(u32x4*)(out + go) = ov;
    if (b == 0 && w == 7) {
      const u32x4 f0 = *(LAS u32x4*)(smem + M_CS + l * 16), f1 = *(LAS u32x4*)(smem + M_CS + 1024 + l * 16);
      const unsigned d = (f0[0] ^ tag[0]) | (f0[1] ^ tag[1]) | (f0[2] ^ tag[0]) | (f0[3] ^ tag[1]) | (f1[0] ^ tag[0]) | (f1[1] ^ tag[1]) | (f1[2] ^ tag[0]) | (f1[3] ^ tag[1]);
      if (!all_lanes(d == 0)) {
#pragma clang loop unroll(disable)
        for (int c = 0; c < 8; ++c) poll(ws, WS_FO + (c * 64 + l) * 4, (l & 1) ? tag[1] : tag[0]); }
      set_flag(tag, wsr, WS_EPOCH);
    }
    asm volatile("Lfk_end:");
    STAMP(9); FT(55);
  }
}
}
#if defined(__HIP_DEVICE_COMPILE__)
#pragma clang attribute pop
#endif

extern "C" void kernel_launch(void* const* d_in, const int* in_sizes, int n_in, void* d_out, int out_size, void* d_ws, size_t ws_size, hipStream_t stream) {
  fk::fused<<<256, 512, 0, stream>>>((const char*)d_in[0], (const char*)d_in[1], (const char*)d_in[2], (const char*)d_in[4], (const char*)d_in[5],
                                     (const char*)d_in[6], (const char*)d_in[7], (char*)d_out, (char*)d_ws);
}
```

```cpp
#include <hip/hip_runtime.h>
#include <stdint.h>
#include <type_traits>

namespace fk {
typedef unsigned short u16;
typedef unsigned long long u64;
typedef short bf16x8 __attribute__((ext_vector_type(8)));
typedef short s16x4 __attribute__((ext_vector_type(4)));
typedef float f32x2 __attribute__((ext_vector_type(2)));
typedef float f32x4 __attribute__((ext_vector_type(4)));
typedef float f32x16 __attribute__((ext_vector_type(16)));
typedef unsigned u32x2 __attribute__((ext_vector_type(2)));
typedef unsigned u32x4 __attribute__((ext_vector_type(4)));
typedef __bf16 bf16x2_t __attribute__((ext_vector_type(2)));
#define LAS __attribute__((address_space(3)))
#define GAS __attribute__((address_space(1)))
#define DI __device__ __forceinline__
typedef LAS char* lptr;

constexpr int S = 2048, D = 512, F = 1792;
constexpr unsigned WS_EPOCH = 0;
constexpr unsigned WS_FH = 32768;
constexpr unsigned WS_FO = WS_FH + 32768;
constexpr unsigned WS_FQKV = WS_FO + 32768;
constexpr unsigned WS_ST = WS_FQKV + 65536;
constexpr unsigned WS_QKV = 1u << 20;
constexpr unsigned WS_HO = WS_QKV + 6u * (1u << 20);
constexpr unsigned WS_END = WS_HO + 32u * 72u * 4096u;
constexpr int SLOT = 16384, NSLOT = 9, MISC = NSLOT * SLOT;
constexpr int M_MASK = MISC, M_B1 = MISC + 1024  , M_B2 = MISC + 1536, M_ONES = MISC + 2048  , M_CNT = MISC + 3072,
              M_TOUCH = MISC + 3328  , M_CS = MISC + 4096  , M_Q = MISC + 8192  ;
constexpr int LDS_BYTES = 163840;

#define MFMA16K16(a, b, c) __builtin_amdgcn_mfma_f32_16x16x16bf16_1k((a), (b), (c), 0, 0, 0)
#define MFMA32(a, b, c) __builtin_amdgcn_mfma_f32_32x32x16_bf16((a), (b), (c), 0, 0, 0)
#define MACC(first, c, a, b) do { if (first) asm volatile("v_mfma_f32_16x16x32_bf16 %0, %1, %2, 0" : "=&v"(c) : "v"(a), "v"(b)); \
                                  else asm volatile("v_mfma_f32_16x16x32_bf16 %0, %1, %2, %0" : "+v"(c) : "v"(a), "v"(b)); } while (0)
#define MEND() asm volatile("s_nop 7" : "+v"(acc[0][0]), "+v"(acc[0][1]), "+v"(acc[0][2]), "+v"(acc[0][3]), "+v"(acc[1][0]), "+v"(acc[1][1]), "+v"(acc[1][2]), "+v"(acc[1][3]))
#define PRIO(n) __builtin_amdgcn_s_setprio(n)
#define PAD() asm volatile("7: s_cbranch_execz 7f")
#define SEG(k) asm volatile("7: s_cbranch_execz Lfk_ret\nLfk_seg" #k ": s_cbranch_execz 7f")
#define IC(v) std::integral_constant<int, (v)>{}
#define HI() do { SBAR(); PRIO(2); SBAR(); } while (0)
#define LO() do { SBAR(); PRIO(0); SBAR(); } while (0)
#define SBAR() __builtin_amdgcn_sched_barrier(0)
#define BAR() asm volatile("s_waitcnt lgkmcnt(0)\n\ts_barrier" ::: "memory")
#define VMW(N) asm volatile("s_waitcnt vmcnt(" #N ")" ::: "memory")
#define RLX_AGENT __ATOMIC_RELAXED, __HIP_MEMORY_SCOPE_AGENT

typedef __amdgpu_buffer_rsrc_t rsrc_t;
DI unsigned rfl(unsigned v) { return __builtin_amdgcn_readfirstlane(v); }
DI rsrc_t mkrsrc(const void* p, unsigned bytes) { return __builtin_amdgcn_make_buffer_rsrc((void*)p, 0, bytes, 0x00020000); }
DI void setm0(unsigned lds_dst) { asm volatile("s_mov_b32 m0, %0\n\ts_nop 0" ::"s"(rfl(lds_dst)) : "memory"); }
template <int OFF> DI void pc16(rsrc_t rs, unsigned voff, unsigned soff) {
  asm volatile("buffer_load_dwordx4 %0, %1, %2 offen offset:%3 lds" ::"v"(voff), "s"(rs), "s"(rfl(soff)), "i"(OFF) : "memory"); }
template <int OFF> DI void pc16s(rsrc_t rs, unsigned voff, unsigned soff) {
  asm volatile("buffer_load_dwordx4 %0, %1, %2 offen offset:%3 sc1 lds" ::"v"(voff), "s"(rs), "s"(rfl(soff)), "i"(OFF) : "memory"); }
DI void item16(rsrc_t rs, const unsigned (&v)[4], unsigned soff, unsigned dst) { setm0(dst); pc16<0>(rs, v[0], soff); pc16<1024>(rs, v[1], soff); pc16<2048>(rs, v[2], soff); pc16<3072>(rs, v[3], soff); }
DI void item16s(rsrc_t rs, unsigned v, unsigned soff, unsigned dst) { setm0(dst); pc16s<0>(rs, v, soff); pc16s<1024>(rs, v, soff); pc16s<2048>(rs, v, soff); pc16s<3072>(rs, v, soff); }
DI void dma4(rsrc_t rs, unsigned voff, unsigned lds_dst) { setm0(lds_dst); asm volatile("buffer_load_dword %0, %1, 0 offen lds" ::"v"(voff), "s"(rs) : "memory"); }
DI void dma4_sc1(rsrc_t rs, unsigned voff, unsigned lds_dst) { setm0(lds_dst); asm volatile("buffer_load_dword %0, %1, 0 offen sc1 lds" ::"v"(voff), "s"(rs) : "memory"); }
template <int OFF> DI void ld16s(bf16x8& d, rsrc_t rs, unsigned voff, unsigned soff) {
  asm volatile("buffer_load_dwordx4 %0, %1, %2, %3 offen offset:%4 sc1" : "=v"(d) : "v"(voff), "s"(rs), "s"(rfl(soff)), "i"(OFF) : "memory"); }
template <int I, int N, class F_> DI void sfor(F_&& f) { if constexpr (I < N) { f(std::integral_constant<int, I>{}); sfor<I + 1, N>(f); } }
DI void st16_sc1(u32x4 v, rsrc_t rs, unsigned voff, unsigned soff) {
  asm volatile("buffer_store_dwordx4 %0, %1, %2, %3 offen sc1\n\ts_nop 1" ::"v"(v), "v"(voff), "s"(rs), "s"(rfl(soff)) : "memory"); }
DI void set_flag(u32x2 tag, rsrc_t rs, unsigned soff) {
  asm volatile("s_mov_b64 exec, 1\n\tbuffer_store_dwordx2 %0, off, %1, %2 sc1\n\ts_mov_b64 exec, -1" ::"v"(tag), "s"(rs), "s"(rfl(soff)) : "memory"); }
DI void count_flag(unsigned cnt_addr, unsigned last, u32x2 tag, rsrc_t rs, unsigned soff) {
  unsigned t;
  asm volatile("s_mov_b64 exec, 1\n\tv_mov_b32 %0, 1\n\tds_add_rtn_u32 %0, %1, %0\n\ts_waitcnt lgkmcnt(0)\n\tv_cmp_eq_u32 vcc, %2, %0\n\ts_cbranch_vccz 1f\n\t"
               "buffer_store_dwordx2 %3, off, %4, %5 sc1\n1:\n\ts_mov_b64 exec, -1"
               : "=&v"(t) : "v"(cnt_addr), "i"(last), "v"(tag), "s"(rs), "s"(rfl(soff)) : "vcc", "memory"); }

DI bf16x8 ld128(lptr p) { return *(LAS bf16x8*)p; }
DI s16x4 ldtr(lptr p) { return __builtin_amdgcn_ds_read_tr16_b64_v4i16((LAS s16x4*)p); }
DI bf16x8 cat8(s16x4 a, s16x4 b) { return __builtin_shufflevector(a, b, 0, 1, 2, 3, 4, 5, 6, 7); }
DI bf16x8 ldtr2(lptr p, int second) { return cat8(ldtr(p), ldtr(p + second)); }
DI unsigned pk(float lo, float hi) { f32x2 f = {lo, hi}; return __builtin_bit_cast(unsigned, __builtin_convertvector(f, bf16x2_t)); }
DI float u2f(unsigned u) { return __builtin_bit_cast(float, u); }
DI unsigned f2u(float f) { return __builtin_bit_cast(unsigned, f); }
DI float bflo(unsigned u) { return u2f(u << 16); }
DI float bfhi(unsigned u) { return u2f(u & 0xffff0000u); }
DI unsigned split2(float v) { const unsigned h = pk(v, 0.f); return pk(v, v - bflo(h)); }
DI lptr lxor(lptr p, unsigned m) { return (lptr)(uintptr_t)((unsigned)(uintptr_t)p ^ m); }
DI float smul(float a, float b) { float d; asm("v_mul_f32 %0, %1, %2" : "=v"(d) : "v"(a), "v"(b)); return d; }
DI float sfma(float a, float b, float c) { float d; asm("v_fma_f32 %0, %1, %2, %3" : "=v"(d) : "v"(a), "v"(b), "v"(c)); return d; }
DI f32x2 pmul(f32x2 a, f32x2 b) { return a * b; }
DI f32x2 pfma(f32x2 a, f32x2 b, f32x2 c) { return __builtin_elementwise_fma(a, b, c); }
template <int C> DI float ror_add(float v) { return v + u2f(__builtin_amdgcn_update_dpp(0, f2u(v), C, 0xf, 0xf, true)); }
DI float rows_add(float v) { auto a = __builtin_amdgcn_permlane16_swap(f2u(v), f2u(v), false, false); v = u2f(a[0]) + u2f(a[1]);
  auto c = __builtin_amdgcn_permlane32_swap(f2u(v), f2u(v), false, false); return u2f(c[0]) + u2f(c[1]); }
DI bool all_lanes(bool c) { return __builtin_amdgcn_ballot_w64(!c) == 0; }

DI void poll(const char* ws, unsigned off, unsigned want) {
  GAS unsigned* p = (GAS unsigned*)(ws + off);
#pragma clang loop unroll(disable)
  for (unsigned spins = 0; spins < (1u << 16); ++spins) {
    if (all_lanes(__hip_atomic_load(p, RLX_AGENT) == want)) break;
    __builtin_amdgcn_s_sleep(16);
  }
  asm volatile("" ::: "memory");
}

typedef unsigned u32x16 __attribute__((ext_vector_type(16)));
DI const char* mkptr(unsigned lo, unsigned hi) { return (const char*)(((u64)hi << 32) | lo); }
__global__ __launch_bounds__(512, 2) void fused(const char*, const char*, const char*, const char*, const char*, const char*, const char*, char*, char*) {
  u32x16 ka; u32x2 kb;
  asm volatile("s_load_dwordx16 %0, %2, 0x0\n\ts_load_dwordx2 %1, %2, 0x40" : "=&s"(ka), "=&s"(kb) : "s"(__builtin_amdgcn_kernarg_segment_ptr()));
  PRIO(3);
  SBAR();
  __shared__ __attribute__((aligned(1024))) char smem_[LDS_BYTES];
  const lptr smem = (lptr)smem_;
  const unsigned lds0 = (unsigned)(uintptr_t)smem;
  const int tid = __builtin_amdgcn_workitem_id_x(), l = tid & 63;
  const int w = __builtin_amdgcn_readfirstlane(tid >> 6);
  const int b = __builtin_amdgcn_workgroup_id_x(), xcd = b & 7, jj = b >> 3;
  { u64 pc; unsigned csz; asm volatile("s_getpc_b64 %0\nLfk_pc:\n\ts_mov_b32 %1, Lfk_end-Lfk_pc" : "=s"(pc), "=s"(csz));
    dma4(mkrsrc((const void*)pc, csz), (unsigned)(jj * 8 + (tid >> 6)) * 128u, lds0 + M_TOUCH); SBAR(); }
  const int r = (xcd & 1) * 8 + (jj & 7), type = jj >> 3, uq = xcd >> 1;
  const int hp = uq * 4 + type;
  const int rh = xcd * 4 + (jj & 3), ct = jj >> 2;
  const bool q1 = type < 2;
  const int kind0 = 1 + (type >> 1), hq = 2 * uq + (type & 1);
  const int f2 = uq * 7 + (q1 ? type : 2 * type - 1), f3 = uq * 7 + 6;
  const int rg = w & 3, ch = w >> 2, m0 = (type ^ ch) & 1;
  const unsigned colsw = (unsigned)(((((l & 7) >> 1) ^ ((l >> 4) & 3)) << 5) + ((l & 1) << 4));
  const unsigned rowL = (unsigned)(8 * (l >> 5) + ((l >> 3) & 3));
  const unsigned ldw = lds0 + rg * 4096;
  unsigned vS[4], vT, sS, dS;
  const bool wq = rg < 2 || q1;
  unsigned pitch = wq ? 3072u : 3584u; pitch = ch ? pitch : 1024u;
  { unsigned sa = kind0 * 1024 + hq * 128, sb = (q1 ? hq : f2 - 1) * 128; asm volatile("" : "+s"(sa), "+s"(sb), "+s"(pitch));
    sa = rg < 2 ? sa : sb; sS = ch ? sa : (unsigned)r * 131072u; dS = ch ? pitch * 64u : 128u;
    const unsigned xs = (unsigned)(((l & 7) ^ (l >> 4)) << 4), r0 = ch ? 32 * (rg & 1) : 32 * rg, r1_ = ch ? 16 : 8, r2_ = ch ? 4 : 16, fl = ch ? 0 : 64;
    const unsigned base = ((ch ? rowL : (unsigned)(l >> 3)) + r0) * pitch, c0 = ch ? colsw : xs, c1 = c0 ^ fl;
    vS[0] = base + c0; vS[1] = base + c1 + (r1_ * pitch - 1024u); vS[2] = base + c0 + (r2_ * pitch - 2048u); vS[3] = base + c1 + ((r1_ + r2_) * pitch - 3072u);
    vT = (unsigned)(32 * r + 16 * (rg & 1) + (l & 15)) * pitch + sS;
    asm volatile("" : "+s"(sS), "+s"(dS), "+v"(vS[0]), "+v"(vS[1]), "+v"(vS[2]), "+v"(vS[3]), "+v"(vT)); }
  const unsigned ldS = ldw + (1 + ch) * SLOT;
  asm volatile("s_waitcnt lgkmcnt(0)" : "+s"(ka), "+s"(kb));
  const char *x = mkptr(ka[0], ka[1]), *wqkv = mkptr(ka[2], ka[3]), *wo = mkptr(ka[4], ka[5]), *w1 = mkptr(ka[6], ka[7]), *b1 = mkptr(ka[8], ka[9]),
             *w2 = mkptr(ka[10], ka[11]), *b2 = mkptr(ka[12], ka[13]);
  char *out = (char*)mkptr(ka[14], ka[15]), *ws = (char*)mkptr(kb[0], kb[1]);
  rsrc_t rS = mkrsrc(ch ? (wq ? wqkv : w1) : x, ch ? 512u * pitch : S * D * 2);
  bf16x8 xr; const unsigned srow = (unsigned)(128 * r + 8 * hp + w);
  const rsrc_t wsr = mkrsrc(ws, WS_END);
  u32x2 tag;
  asm volatile("buffer_load_dwordx2 %0, off, %1, 0 sc1" : "=v"(tag) : "s"(wsr) : "memory");
  ld16s<0>(xr, mkrsrc(x, S * D * 2), (unsigned)l * 16u, srow * 1024u);
  item16(rS, vS, sS, ldS);
  PRIO(2);
  item16(rS, vS, sS + dS, ldS + 2 * SLOT);
  PRIO(0);
  if (ch) dma4(rS, vT, lds0 + M_TOUCH);
  sS += 2 * dS;
  const rsrc_t r1 = mkrsrc(w1, D * F * 2);
  const int t16 = l & 15, g4 = l >> 4;
  if (w == 7) { const unsigned o1 = 4 * r - 8 + t16 >= 0 ? 0x3f803f80u : 0u; *(LAS u32x2*)(smem + M_ONES + g4 * 256 + t16 * 8) = u32x2{o1, o1}; *(LAS unsigned*)(smem + M_CNT + l * 4) = 0u; }
  const int aB0 = (4 * g4 + ((l >> 2) & 3)) * 128 + 8 * (l & 3) + ((((g4 & 1) << 1) | ((l >> 3) & 1)) << 5);
  const int aX0 = (32 * rg + 16 * m0 + t16) * 128 + ((g4 ^ (t16 >> 1)) << 4);
  bf16x8 ones = {0x3f80, 0x3f80, 0x3f80, 0x3f80, 0x3f80, 0x3f80, 0x3f80, 0x3f80};
  bf16x8 xf[2][16];
  f32x4 acc[2][4], cs;
  s16x4 h[2][4][2], cor[2];
  float rstd_[2], std_[2];
  auto put_cs = [&](int buf, unsigned bv) {
    const unsigned hl = split2(g4 == 0 ? cs[0] : 0.f);
    *(LAS u32x2*)(smem + M_CS + (g4 < 2 ? buf * 1024 + g4 * 512 : 3072) + (16 * rg + t16) * 8) = u32x2{hl, (hl & 0xffffu) | (bv << 16)}; };
  auto bias16 = [&](int boff, bool on) -> unsigned { const unsigned v = *(LAS u16*)(smem + M_B1 + boff + 2 * (16 * rg + t16)); return on && g4 < 2 ? v : 0u; };
  s16x4 fa[4];
  auto fix_rd = [&](int buf) {
#pragma unroll
    for (int n = 0; n < 4; ++n) fa[n] = *(LAS s16x4*)(smem + M_CS + (g4 < 2 ? buf * 1024 + g4 * 512 : 3072) + ((128 * rg + t16 * 8) ^ (128 * n)));
    SBAR(); };
  auto fix_mm = [&]() {
#pragma unroll
    for (int n = 0; n < 4; ++n) { acc[0][n] = MFMA16K16(fa[n], cor[0], acc[0][n]); acc[1][n] = MFMA16K16(fa[n], cor[1], acc[1][n]); }
    SBAR(); };
  auto meet4 = [&](int word) {
    asm volatile("s_waitcnt lgkmcnt(0)\n\ts_mov_b64 exec, 1\n\tds_add_u32 %0, %1\n\ts_mov_b64 exec, -1" ::"v"(lds0 + M_CNT + word), "v"(1u) : "memory");
#pragma clang loop unroll(disable)
    for (unsigned spins = 0; spins < (1u << 22); ++spins) { if (rfl(*(volatile LAS unsigned*)(smem + M_CNT + word)) == 4u) break; }
    asm volatile("" ::: "memory"); };
  auto flags_get = [&](unsigned off) { dma4_sc1(wsr, off, lds0 + M_Q + w * 1024); };
  auto flags_chk = [&](unsigned off) { const unsigned want = (l & 1) ? tag[1] : tag[0];
    if (__builtin_expect(!all_lanes(*(LAS unsigned*)(smem + M_Q + w * 1024 + l * 4) == want), 0)) poll(ws, off, want); };
  auto gelu8 = [&](f32x4 a, f32x4 c, int m) -> u32x4 {
    const float c1 = -2.f * 0.7978845608028654f * 1.4426950408889634f, k1 = c1 * rstd_[m], k2 = (c1 * 0.044715f) * rstd_[m] * rstd_[m] * rstd_[m], sd = std_[m];
    float v[8], y[8];
    asm volatile("s_nop 7" : "+v"(a), "+v"(c));
#pragma unroll
    for (int e = 0; e < 8; ++e) { v[e] = e < 4 ? a[e & 3] : c[e & 3]; y[e] = smul(v[e], v[e]); }
    SBAR();
#pragma unroll
    for (int e = 0; e < 8; ++e) y[e] = sfma(y[e], k2, k1);
    SBAR();
#pragma unroll
    for (int e = 0; e < 8; ++e) y[e] = smul(y[e], v[e]);
    SBAR();
#pragma unroll
    for (int e = 0; e < 8; ++e) y[e] = __builtin_amdgcn_exp2f(y[e]);
    SBAR(); asm volatile("s_nop 0"); SBAR();
#pragma unroll
    for (int e = 0; e < 8; ++e) y[e] = sfma(y[e], sd, sd);
    SBAR();
#pragma unroll
    for (int e = 0; e < 8; ++e) y[e] = __builtin_amdgcn_rcpf(y[e]);
    SBAR(); asm volatile("s_nop 0"); SBAR();
#pragma unroll
    for (int e = 0; e < 8; ++e) y[e] = smul(y[e], v[e]);
    SBAR();
    return u32x4{pk(y[0], y[1]), pk(y[2], y[3]), pk(y[4], y[5]), pk(y[6], y[7])}; };
  auto gelu_pair = [&](int m, int kk) -> u32x4 { const u32x4 v = gelu8(acc[m][2 * kk], acc[m][2 * kk + 1], m); return (rg & 1) ? u32x4{v[2], v[3], v[0], v[1]} : v; };
  auto gelu16p = [&](int m, u32x4& o0, u32x4& o1) {
    const float c1 = -2.f * 0.7978845608028654f * 1.4426950408889634f, k1 = c1 * rstd_[m], k2 = (c1 * 0.044715f) * rstd_[m] * rstd_[m] * rstd_[m];
    f32x2 K1 = {k1, k1}, K2 = {k2, k2}, SD = {std_[m], std_[m]}, v[8], y[8];
#pragma unroll
    for (int e = 0; e < 8; ++e) { v[e] = f32x2{acc[m][e >> 1][2 * (e & 1)], acc[m][e >> 1][2 * (e & 1) + 1]}; y[e] = pmul(v[e], v[e]); }
    SBAR();
#pragma unroll
    for (int e = 0; e < 8; ++e) y[e] = pfma(y[e], K2, K1);
    SBAR();
#pragma unroll
    for (int e = 0; e < 8; ++e) y[e] = pmul(y[e], v[e]);
    SBAR();
#pragma unroll
    for (int e = 0; e < 8; ++e) y[e] = f32x2{__builtin_amdgcn_exp2f(y[e][0]), __builtin_amdgcn_exp2f(y[e][1])};
    SBAR();
#pragma unroll
    for (int e = 0; e < 8; ++e) y[e] = pfma(y[e], SD, SD);
    SBAR();
#pragma unroll
    for (int e = 0; e < 8; ++e) y[e] = f32x2{__builtin_amdgcn_rcpf(y[e][0]), __builtin_amdgcn_rcpf(y[e][1])};
    SBAR();
#pragma unroll
    for (int e = 0; e < 8; ++e) y[e] = pmul(y[e], v[e]);
    SBAR();
    unsigned p[8];
#pragma unroll
    for (int e = 0; e < 8; ++e) { p[e] = pk(y[e][0], y[e][1]); asm volatile("" : "+v"(p[e])); }
    o0 = (rg & 1) ? u32x4{p[2], p[3], p[0], p[1]} : u32x4{p[0], p[1], p[2], p[3]}; o1 = (rg & 1) ? u32x4{p[6], p[7], p[4], p[5]} : u32x4{p[4], p[5], p[6], p[7]}; };
  constexpr unsigned QF = 128 * 3584;

  {
    int wb4[4], xb4[4];
#pragma unroll
    for (int n = 0; n < 4; ++n) { wb4[n] = (aB0 ^ (32 * (n ^ rg))) + 2 * SLOT + ch * 8192; xb4[n] = (aX0 ^ (64 * (n >> 1)) ^ (2048 * (n & 1))) + SLOT;
      asm volatile("" : "+v"(wb4[n]), "+v"(xb4[n])); }
    asm volatile("" : "+v"(ones));
    bf16x8 sg[2]; u32x4 gr; unsigned bv1, psel = g4 == 0 ? 0x05040302u : 0x07060302u;
    int pca;
    const unsigned sgo = (unsigned)(32 * rg + 16 * m0 + t16) * 16u;
    { asm volatile("s_waitcnt vmcnt(8)" : "+v"(xr), "+v"(tag) :: "memory"); const u32x4 u = __builtin_bit_cast(u32x4, xr);
      float v[8];
#pragma unroll
      for (int e = 0; e < 4; ++e) { v[2 * e] = bflo(u[e]); v[2 * e + 1] = bfhi(u[e]); }
      auto wsum = [](float t) { return rows_add(ror_add<0x121>(ror_add<0x122>(ror_add<0x124>(ror_add<0x128>(t))))); };
      const float smean = wsum(((v[0] + v[1]) + (v[2] + v[3])) + ((v[4] + v[5]) + (v[6] + v[7]))) * (-1.f / D);
      float q = 0.f;
#pragma unroll
      for (int e = 0; e < 8; ++e) { const float d = v[e] + smean; q = __builtin_fmaf(d, d, q); }
      const u64 t = (((u64)tag[1] << 32) | tag[0]) + 1; tag = u32x2{(unsigned)t, (unsigned)(t >> 32)};
      gr = u32x4{split2(smean), tag[0], split2(__builtin_amdgcn_sqrtf(__builtin_fmaf(wsum(q), 1.f / D, 1e-5f))), tag[0]};
      asm volatile("" : "+v"(psel), "+v"(gr), "+v"(tag)); }
    if (ch) VMW(5); else VMW(4); BAR();
    xf[0][0] = ld128(smem + xb4[0]); xf[1][0] = ld128(smem + xb4[1]);
#pragma unroll
    for (int n = 0; n < 4; ++n) { h[0][n][0] = ldtr(smem + wb4[n]); h[0][n][1] = ldtr(smem + wb4[n] + 2048); }
    sfor<0, 16>([&](auto kc) {
      constexpr int k = decltype(kc)::value, kn = k + 1, c = k & 1, hs = kn >> 1;
      if constexpr ((k & 1) && k < 15) {
        if constexpr (hs == 6) { rS = r1; sS = (unsigned)f2 * 128u + ch * QF; dS = 2 * QF; }
        if (hs > 1) VMW(4); else if (ch) VMW(6); else VMW(5); BAR();
      }
      if constexpr (k == 12) { if (rg == 2) { const int nl = ch ? 16 : 56;
        dma4(mkrsrc(ch ? wo : w2, nl * 32768), (unsigned)(jj * nl + (l < nl ? l : 0)) * 1024u + ((384u - (ch ? ka[4] : ka[10])) & 0x380u), lds0 + M_TOUCH); } }
      if constexpr (k == 2) {
        if (w == 4) dma4(r1, (unsigned)(b * 56 + (l < 56 ? l : 0)) * 128u, lds0 + M_TOUCH);
        if (w == 3) dma4_sc1(wsr, (l & 3) == 0 ? WS_FH + r * 2048 + hp * 128 : (l & 3) == 1 ? WS_FO + r * 2048 + hp * 128 : WS_FQKV + r * 4096 + (hq * 3 + ((l & 1) ? 0 : kind0)) * 128, lds0 + M_TOUCH);
        if (w == 5) dma4(mkrsrc(b1, F * 2), (unsigned)(l * 4 + (f2 - 1) * 128), lds0 + M_B1);
        if (w == 6) dma4(mkrsrc(b1, F * 2), (unsigned)(l * 4 + f3 * 128), lds0 + M_B1 + 256);
        if (w == 7) dma4(mkrsrc(b2, D * 2), (unsigned)(l * 4 + ct * 128), lds0 + M_B2); }
      if constexpr (k == 6) { if (rg == 3) { const int nl = ch ? 16 : 56; dma4(mkrsrc(ch ? wo : w2, nl * 32768), (unsigned)(b * nl + (l < nl ? l : 0)) * 128u, lds0 + M_TOUCH); } }
      PRIO(k & 1);
      constexpr int so_ = (hs & 1) * 2 * SLOT;
      if constexpr (k == 0 || ((k & 1) && k < 15)) setm0(hs < 6 ? ldS + ((hs + 2) & 3) * 2 * SLOT : ldS + (2 * hs - 13) * SLOT);
      sfor<0, 4>([&](auto nc) { constexpr int n = decltype(nc)::value;
        const bf16x8 bf = cat8(h[c][n][0], h[c][n][1]);
        if constexpr (k < 15) { h[c ^ 1][n][0] = ldtr(smem + wb4[n] + so_ + (kn & 1) * 4096); if constexpr (n < 2) xf[n][kn] = ld128(smem + xb4[2 * (kn & 1) + n] + so_); }
        SBAR(); MACC(k == 0, acc[0][n], bf, xf[0][k]); SBAR();
        if constexpr (k < 15) h[c ^ 1][n][1] = ldtr(smem + wb4[n] + so_ + (kn & 1) * 4096 + 2048);
        if constexpr (k == 0 || ((k & 1) && k < 15)) pc16<1024 * n>(rS, vS[n], sS);
        SBAR(); MACC(k == 0, acc[1][n], bf, xf[1][k]); SBAR();
        if constexpr (n == 0) MACC(k == 0, cs, ones, bf);
        if constexpr (k == 0 && n == 3) asm volatile("s_mov_b64 exec, 1\n\tbuffer_store_dwordx4 %0, off, %1, %2 sc1\n\ts_mov_b64 exec, -1\n\ts_nop 0" ::"v"(gr), "s"(wsr), "s"(WS_ST + srow * 16u) : "memory");
        if constexpr (k == 10 && n < 2) ld16s<0>(sg[n], wsr, sgo ^ (256u * n), WS_ST + r * 2048);
        if constexpr (k == 10) { vS[n] = (rowL + 32 * rg + 16 * (n & 1) + 4 * (n >> 1)) * 3584u + colsw - 1024u * n; asm volatile("" : "+v"(vS[n])); }
        if constexpr (k == 13 && n == 3) {
          asm volatile("" : "+v"(sg[0]), "+v"(sg[1]));
#pragma clang loop unroll(disable)
          for (unsigned spins = 0; spins < (1u << 16); ++spins) { const u32x4 a_ = __builtin_bit_cast(u32x4, sg[0]), b_ = __builtin_bit_cast(u32x4, sg[1]);
            if (__builtin_expect(all_lanes(((a_[1] ^ tag[0]) | (a_[3] ^ tag[0]) | (b_[1] ^ tag[0]) | (b_[3] ^ tag[0])) == 0), 1)) break;
            asm volatile("buffer_load_dwordx4 %0, %2, %4, %5 offen sc1\n\tbuffer_load_dwordx4 %1, %3, %4, %5 offen sc1\n\ts_waitcnt vmcnt(0)"
                         : "=&v"(sg[0]), "=&v"(sg[1]) : "v"(sgo), "v"(sgo ^ 256u), "s"(wsr), "s"(WS_ST + r * 2048) : "memory"); } }
        if constexpr (k == 14 && (n & 1)) { constexpr int m = n >> 1; const u32x4 a_ = __builtin_bit_cast(u32x4, sg[m]);
          std_[m] = bflo(a_[2]) + bfhi(a_[2]); rstd_[m] = __builtin_amdgcn_rcpf(std_[m]);
          cor[m] = __builtin_bit_cast(s16x4, u32x2{__builtin_amdgcn_perm(a_[0], a_[0], 0x01000100u), __builtin_amdgcn_perm(a_[2], a_[0], psel)});
          asm volatile("" : "+v"(cor[m]), "+v"(rstd_[m]), "+v"(std_[m])); }
        if constexpr (k == 15) { vS[n] = (unsigned)(64 * rg + 32 * (n >> 1) + 4 * (n & 1) + 8 * g4 + ((l >> 2) & 3)) * 3584u + ((((l >> 1) ^ g4) & 1) << 5) + ((l & 1) << 4) - 1024u * n; asm volatile("" : "+v"(vS[n])); }
        if constexpr (k == 8 && n == 0) { bv1 = bias16(0, ch && !q1); pca = M_CS + (g4 < 2 ? ch * 1024 + g4 * 512 : 3072) + (16 * rg + t16) * 8; }
        if constexpr (k == 8 && n == 3) { bv1 <<= 16; asm volatile("" : "+v"(bv1), "+v"(pca)); }
        if constexpr ((k & 3) == 2 && k < 14) { const int d = k == 6 ? -4 * SLOT : 4 * SLOT; wb4[n] += d; asm volatile("" : "+v"(wb4[n])); if constexpr (n >= 2) { xb4[2 * (n - 2)] += d; xb4[2 * (n - 2) + 1] += d; asm volatile("" : "+v"(xb4[2 * (n - 2)]), "+v"(xb4[2 * (n - 2) + 1])); } }
        SBAR(); });
      if constexpr ((k & 1) == 0) sS += dS;
    });
    PRIO(0);
    asm volatile("s_nop 1" : "+v"(cs));
    { const unsigned hl = split2(g4 == 0 ? cs[0] : 0.f); *(LAS u32x2*)(smem + pca) = u32x2{hl, (hl & 0xffffu) | bv1}; }
    MEND();
    VMW(0); BAR();
    if (ch && q1) {
      const unsigned s3 = (unsigned)(f3 * 128 + type * 64);
      item16(r1, vS, s3, ldw + 4 * SLOT); item16(r1, vS, s3 + 2 * QF, ldw + 5 * SLOT); }
  }
  const unsigned voffL = (unsigned)l * 16u;
  const unsigned qsrc = WS_QKV + 2 * hp * 65536;
  const unsigned fq = WS_FQKV + r * 4096 + (hp >> 1) * 384 + (l & 1) * 4;
  const unsigned hbase = WS_HO + (unsigned)((2 * r + (rg >> 1)) * 288 + 2 * (rg & 1) + m0) * 1024u;
  int ac[4];
#pragma unroll
  for (int n = 0; n < 4; ++n) { ac[n] = aB0 ^ (32 * (n ^ rg)); asm volatile("" : "+v"(ac[n])); }
  fix_rd(ch);
  if (ch == 0) {
#pragma unroll
    for (int n = 0; n < 4; ++n) { h[0][n][0] = ldtr(smem + ac[n]); h[0][n][1] = ldtr(smem + ac[n] + 2048); } }
  fix_mm();
  const int tok = (l & 31) ^ (16 * m0);
  const unsigned qlo = !ch && kind0 == 2 ? tok * 32 + (l >> 5) * 16 : (l >> 5) * 512 + tok * 16, qso = WS_QKV + (unsigned)((ch ? 0 : kind0) * 2048 + hq * 256 + 4 * r + rg) * 1024u;
  if (ch == 0 || q1) {
    const float sc = ch ? 0.25f * 1.4426950408889634f : 1.f, rs0 = rstd_[0] * sc, rs1 = rstd_[1] * sc;
    MEND();
#pragma unroll
    for (int n = 0; n < 4; ++n) {
      const f32x4 y0 = {smul(acc[0][n][0], rs0), smul(acc[0][n][1], rs0), smul(acc[0][n][2], rs0), smul(acc[0][n][3], rs0)};
      const f32x4 y1 = {smul(acc[1][n][0], rs1), smul(acc[1][n][1], rs1), smul(acc[1][n][2], rs1), smul(acc[1][n][3], rs1)};
      auto s0 = __builtin_amdgcn_permlane16_swap(pk(y0[0], y0[1]), pk(y1[0], y1[1]), false, false);
      auto s1 = __builtin_amdgcn_permlane16_swap(pk(y0[2], y0[3]), pk(y1[2], y1[3]), false, false);
      st16_sc1(u32x4{s0[0], s1[0], s0[1], s1[1]}, wsr, qlo, qso + (n ^ rg) * 65536u); }
  }
  if (ch == 0) {
    PRIO(3);
    unsigned bv;
#pragma unroll
    for (int k = 0; k < 16; ++k) {
      const int kn = k + 1, c = k & 1;
      if (k == 5) { VMW(0); count_flag(lds0 + M_CNT + 8, 3, tag, wsr, WS_FQKV + r * 4096 + (hq * 3 + kind0) * 128); PRIO(1); }
#pragma unroll
      for (int n = 0; n < 4; ++n) {
        const bf16x8 bf = cat8(h[c][n][0], h[c][n][1]);
        if (k < 15) h[c ^ 1][n][0] = ldtr(smem + ac[n] + kn * 4096);
        SBAR(); MACC(k == 0, acc[0][n], bf, xf[0][k]); SBAR();
        if (k < 15) h[c ^ 1][n][1] = ldtr(smem + ac[n] + kn * 4096 + 2048);
        SBAR(); MACC(k == 0, acc[1][n], bf, xf[1][k]); SBAR();
        if (n == 0) MACC(k == 0, cs, ones, bf);
        if (k == 15 && n == 0) bv = bias16(128, true);
      }
    }
    asm volatile("s_nop 1" : "+v"(cs));
    put_cs(2, bv);
    MEND();
    PRIO(2);
    meet4(32);
    fix_rd(2);
  }
  const rsrc_t rw2 = mkrsrc(w2, F * D * 2);
  int fr = r - 2 + ((l >> 1) & 3); fr = fr < 0 ? 0 : fr > r ? r : fr;
  const unsigned fkv = WS_FQKV + fr * 4096 + ((hp >> 1) * 3 + 1 + (rg >> 1)) * 128 + (l & 1) * 4;
  const unsigned kvs = qsrc + ((rg & 1) + 32 + 32 * (rg >> 1)) * 65536 + r * 4096, kvd = lds0 + 6 * SLOT + rg * 12288;
  auto kv_own = [&]() { flags_chk(fkv); item16s(wsr, voffL, kvs, kvd + 8192); };
  auto kv_old = [&]() { flags_get(fq); item16s(wsr, voffL, r < 1 ? WS_END : kvs - 4096, kvd + 4096); item16s(wsr, voffL, r < 2 ? WS_END : kvs - 8192, kvd); };
  auto q_get = [&]() { flags_chk(fq);
    setm0(lds0 + M_Q + rg * 1024); pc16s<0>(wsr, voffL, qsrc + (4 * r + rg) * 1024); setm0(lds0 + M_Q + w * 1024); pc16s<0>(wsr, voffL, qsrc + 65536 + (4 * r + rg) * 1024); };
  unsigned vD[4];
#pragma unroll
  for (int c = 0; c < 4; ++c) vD[c] = (unsigned)((32 * rg + (l >> 3)) * 1024 + c * 7168 + ct * 128) + colsw;
  {
    unsigned hs = hbase + (unsigned)(f2 - ch) * 8192u + (rg >> 1) * 4096;
    if (ch && q1) {
      PRIO(2);
      VMW(4); meet4(36);
      const lptr pa = smem + 4 * SLOT + (4 * g4 + ((l >> 2) & 3)) * 64 + (((g4 ^ rg) & 1) << 5) + 8 * (l & 3), pb = lxor(pa, 32);
      const lptr bp = smem + M_B1 + 256 + 64 * type + 32 * (rg & 1) + 2 * t16;
      unsigned ba = *(LAS u16*)bp, bb = *(LAS u16*)lxor(bp, 32);
      f32x4 ca, cb;
      s16x4 g[2][4];
      g[0][0] = ldtr(pa); g[0][1] = ldtr(pa + 1024); g[0][2] = ldtr(pb); g[0][3] = ldtr(pb + 1024);
#pragma unroll
      for (int k = 0; k < 16; ++k) {
        const bf16x8 ga = cat8(g[k & 1][0], g[k & 1][1]), gb_ = cat8(g[k & 1][2], g[k & 1][3]);
        const int kn = k + 1, c = kn & 1;
        if (k == 5) { VMW(0); count_flag(lds0 + M_CNT + 12, 3, tag, wsr, WS_FQKV + r * 4096 + hq * 384); }
        if (k == 7) flags_get(fkv);
        if (k == 11) { VMW(0); kv_own(); }
        if (kn < 16) g[c][0] = ldtr(pa + kn * 2048);
        SBAR(); MACC(k == 0, acc[0][2], ga, xf[0][k]); SBAR();
        if (kn < 16) g[c][1] = ldtr(pa + kn * 2048 + 1024);
        SBAR(); MACC(k == 0, acc[0][3], gb_, xf[0][k]); SBAR();
        if (kn < 16) g[c][2] = ldtr(pb + kn * 2048);
        SBAR(); MACC(k == 0, acc[1][2], ga, xf[1][k]); SBAR();
        if (kn < 16) g[c][3] = ldtr(pb + kn * 2048 + 1024);
        SBAR(); MACC(k == 0, acc[1][3], gb_, xf[1][k]); MACC(k == 0, ca, ones, ga); MACC(k == 0, cb, ones, gb_); SBAR();
      }
      asm volatile("s_nop 7" : "+v"(acc[0][2]), "+v"(acc[0][3]), "+v"(acc[1][2]), "+v"(acc[1][3]), "+v"(ca), "+v"(cb));
      ba = l < 32 ? ba << 16 : 0; bb = l < 32 ? bb << 16 : 0;
      const unsigned ha = split2(l < 16 ? ca[0] : 0.f), hb = split2(l < 16 ? cb[0] : 0.f);
      const s16x4 fa_ = __builtin_bit_cast(s16x4, u32x2{ha, (ha & 0xffffu) | ba}), fb_ = __builtin_bit_cast(s16x4, u32x2{hb, (hb & 0xffffu) | bb});
#pragma unroll
      for (int m = 0; m < 2; ++m) { acc[m][2] = MFMA16K16(fa_, cor[m], acc[m][2]); acc[m][3] = MFMA16K16(fb_, cor[m], acc[m][3]); }
      hs = (hbase + (unsigned)f3 * 8192u + type * 4096) ^ 4096u;
      LO(); kv_old();
    } else if (ch) {
      st16_sc1(gelu_pair(0, 0), wsr, voffL, hs);
      SBAR();
      st16_sc1(gelu_pair(1, 0), wsr, voffL, hs ^ 1024u);
      SBAR();
    }
    if (ch) {
      st16_sc1(gelu_pair(0, 1), wsr, voffL, hs ^ 4096u);
      SBAR();
      if (q1) { VMW(9); HI(); q_get(); } else flags_get(fkv);
      LO();
      st16_sc1(gelu_pair(1, 1), wsr, voffL, hs ^ 5120u);
      SBAR(); }
    if (ch == 0) { fix_mm();
#pragma unroll
      for (int m = 0; m < 2; ++m) { u32x4 o0, o1; gelu16p(m, o0, o1); st16_sc1(o0, wsr, voffL, hs ^ (1024u * m)); st16_sc1(o1, wsr, voffL, hs ^ (4096u + 1024u * m)); SBAR(); }
      item16(rw2, vD, 0, ldw); item16(rw2, vD, 131072, ldw + SLOT); }
    else { HI();
      if (!q1) { VMW(1); kv_own(); kv_old(); VMW(8); q_get(); }
#pragma clang loop unroll(disable)
      for (unsigned spins = 0; spins < (1u << 22); ++spins) { if (rfl(*(volatile LAS unsigned*)(smem + M_CNT + 8)) == 4u) break; }
      const int z = (l & 31) - 4 * (l >> 5) - 8 * rg;
      *(LAS u32x2*)(smem + (rg < 2 ? M_CS : M_MASK) + l * 16 + (rg & 1) * 8) = u32x2{1 <= z ? 0xffffffffu : 0 <= z ? 0xffffu : 0u, 3 <= z ? 0xffffffffu : 2 <= z ? 0xffffu : 0u};
      if (q1) VMW(1); else VMW(0); }
  }
  bf16x8 bq[6][4];
  const rsrc_t rwo = mkrsrc(wo, D * D * 2);
  int ah[4];
#pragma unroll
  for (int n = 0; n < 4; ++n) { ac[n] = (aB0 ^ (32 * n)) + rg * 4096; ah[n] = ac[n] + 3 * SLOT; asm volatile("" : "+v"(ac[n]), "+v"(ah[n])); }
  auto bcell = [&](int it, int n) -> lptr { const int s_ = it % 6; return smem + (s_ < 3 ? ac[n] : ah[n]) + (s_ % 3) * SLOT; };
  auto bpc = [&](auto itc, auto qc) { constexpr int it = decltype(itc)::value, q = decltype(qc)::value;
    if (q == 0) { setm0(ldw + (it % 6) * SLOT); if (it == 14) { vD[1] += 8192; vD[2] -= 8192; } }
    pc16<1024 * q>(it < 14 ? rw2 : rwo, vD[q], (it < 14 ? it : it - 14) * 131072u); };
  const unsigned fall = (unsigned)((rh >> 1) * 2048 + ((l >> 1) & 15) * 128 + (l & 1) * 4);
  {
    const int i = rg, hh = l >> 5, iq = l & 31;
    const lptr kp = smem + 6 * SLOT + ch * 12288 + i * 1024 + l * 16;
    const bool onesl = (l & 16) != 0;
    const lptr vp8 = onesl ? smem + M_ONES + 8 * (i + 8) : smem + 6 * SLOT + 24576 + ch * 12288 + (i + 8) * 1024 + (4 * hh + (t16 >> 2)) * 32 + (l & 3) * 8;
    const int vstep = onesl ? 8 : 1024;
    const unsigned ovo = (unsigned)((iq >> 4) * 1024 + ((iq & 15) + 16 * (2 * ch + hh)) * 16);
    BAR();
    PRIO(0);
    const bf16x8 qf = ld128(smem + M_Q + w * 1024 + l * 16), k8 = ld128(kp + 8 * 1024);
    bf16x8 kn = ld128(kp + 7 * 1024);
    const u32x4 mka = *(LAS u32x4*)(smem + M_CS + l * 16), mkb = *(LAS u32x4*)(smem + M_MASK + l * 16);
    const unsigned mk[8] = {mka[0], mka[1], mka[2], mka[3], mkb[0], mkb[1], mkb[2], mkb[3]};
    const f32x16 zero = {};
    float m;
    { const f32x16 s = MFMA32(k8, qf, zero); m = u2f(__builtin_amdgcn_permlane32_swap(f2u(s[0]), f2u(s[0]), false, false)[0]); }
    f32x16 o, nm;
#pragma unroll
    for (int c = 0; c < 16; ++c) nm[c] = -m;
    lptr vp = vp8;
    f32x16 sc = MFMA32(k8, qf, nm);
    sfor<0, 9>([&](auto jc) {
      constexpr int j = decltype(jc)::value, bb = 8 - j;
      const bf16x8 v0 = ldtr2(vp, 256), v1 = ldtr2(vp + 512, 256);
      f32x16 sn;
      if constexpr (bb > 0) sn = MFMA32(kn, qf, nm);
      if constexpr (bb > 1) kn = ld128(kp + (bb - 2) * 1024);
      vp -= vstep;
      if constexpr (j == 1) { if (ch) VMW(0); else VMW(16); count_flag(lds0 + M_CNT, 7, tag, wsr, WS_FH + r * 2048 + hp * 128); }
      if (ch == 0) {
        if constexpr (j == 0) sfor<0, 4>([&](auto qc) { bpc(IC(2), qc); });
        if constexpr (j >= 2 && j < 8) { VMW(16);
#pragma unroll
          for (int n = 0; n < 4; ++n) bq[j - 2][n] = ldtr2(bcell(j - 2, n), 2048); }
        if constexpr (j == 8) flags_get(WS_FH + fall);
      }
      SBAR(); SBAR();
      unsigned p[8];
#pragma unroll
      for (int c = 0; c < 4; ++c) { p[c] = pk(__builtin_amdgcn_exp2f(sc[2 * c]), __builtin_amdgcn_exp2f(sc[2 * c + 1])); if (bb == 8) p[c] &= mk[c]; if (bb == 0) p[c] &= ~mk[c]; }
      o = MFMA32(v0, __builtin_bit_cast(bf16x8, u32x4{p[0], p[1], p[2], p[3]}), bb == 8 ? zero : o);
      SBAR();
      if (ch == 0) sfor<0, 4>([&](auto qc) { bpc(IC(j + 3), qc); });
      SBAR();
#pragma unroll
      for (int c = 4; c < 8; ++c) { p[c] = pk(__builtin_amdgcn_exp2f(sc[2 * c]), __builtin_amdgcn_exp2f(sc[2 * c + 1])); if (bb == 8) p[c] &= mk[c]; if (bb == 0) p[c] &= ~mk[c]; }
      o = MFMA32(v1, __builtin_bit_cast(bf16x8, u32x4{p[4], p[5], p[6], p[7]}), o);
      asm volatile("s_waitcnt lgkmcnt(0)" ::: "memory");
      SBAR();
      sc = sn;
    });
    if (__builtin_expect(!all_lanes(o[8] < 1.0995116e12f && o[8] > 9.094947e-13f), 0)) {
      int bstart = 8 - 4 * r - i; bstart = bstart < 0 ? 0 : bstart;
#pragma clang loop unroll(disable)
      for (int pass = 0; pass < 2; ++pass) {
        o = zero;
#pragma clang loop unroll(disable)
        for (int bb = bstart; bb <= 8; ++bb) { const f32x16 s = MFMA32(ld128(kp + bb * 1024), qf, zero);
          const unsigned fl = bb == 0 ? 0xffffffffu : 0u, al = (bb == 0 || bb == 8) ? 0u : 0xffffffffu;
          const lptr vq = vp8 - (8 - bb) * vstep;
          unsigned p[8];
#pragma unroll
          for (int c = 0; c < 8; ++c) { const unsigned keep = (mk[c] ^ fl) | al;
            p[c] = pk(__builtin_amdgcn_exp2f(s[2 * c] - m), __builtin_amdgcn_exp2f(s[2 * c + 1] - m)) & keep;
            if (pass == 0) { m = (keep & 1u) ? __builtin_fmaxf(m, s[2 * c]) : m; m = (keep >> 31) ? __builtin_fmaxf(m, s[2 * c + 1]) : m; } }
          o = MFMA32(ldtr2(vq, 256), __builtin_bit_cast(bf16x8, u32x4{p[0], p[1], p[2], p[3]}), o);
          o = MFMA32(ldtr2(vq + 512, 256), __builtin_bit_cast(bf16x8, u32x4{p[4], p[5], p[6], p[7]}), o); }
        { auto rr = __builtin_amdgcn_permlane32_swap(f2u(m), f2u(m), false, false); m = __builtin_fmaxf(u2f(rr[0]), u2f(rr[1])); }
      }
    }
    if (ch) PRIO(3);
    const float rl = __builtin_amdgcn_rcpf(o[8]);
    const u32x4 ov = {pk(o[0] * rl, o[1] * rl), pk(o[2] * rl, o[3] * rl), pk(o[4] * rl, o[5] * rl), pk(o[6] * rl, o[7] * rl)};
    st16_sc1(ov, wsr, ovo, WS_HO + (unsigned)(((2 * r + (i >> 1)) * 72 + 56 + hp) * 4 + 2 * (i & 1)) * 1024u);
  }
  const unsigned gs = (unsigned)((rh * 64 + 8 * w) * 1024 + ct * 128);
  auto gv = [&]() -> unsigned { unsigned v = voffL; asm volatile("" : "+v"(v)); return ((v & 0x380u) << 3) | (v & 0x70u); };
  u32x4 xv;
  if (ch) {
      asm volatile("s_mov_b64 exec, 0\n\ts_cmp_eq_u32 %0, 0\n\ts_cbranch_scc1 Lfk_seg0\n\ts_cmp_eq_u32 %0, 1\n\ts_cbranch_scc1 Lfk_seg1\n\ts_cmp_eq_u32 %0, 2\n\ts_cbranch_scc1 Lfk_seg2\n\t"
                   "s_branch Lfk_seg3\nLfk_ret:\n\ts_mov_b64 exec, -1" ::"s"(rg) : "scc");
    VMW(0);
    count_flag(lds0 + M_CNT + 4, 7, tag, wsr, WS_FO + r * 2048 + hp * 128);
    xv = *(const GAS u32x4*)(x + gs + gv());
    __builtin_amdgcn_s_sleep(40);
    poll(ws, WS_FO + (rh >> 1) * 2048 + (((l >> 1) & 3) * 4 + rg) * 128 + (l & 1) * 4, (l & 1) ? tag[1] : tag[0]);
    *(volatile LAS unsigned*)(smem + M_CNT + 16 + rg * 4) = 1u;
    if (b == 0 && w == 7) {
#pragma clang loop unroll(disable)
      for (unsigned spins = 0; spins < (1u << 16); ++spins) {
        bf16x8 f[4]; unsigned d = 0;
#pragma unroll
        for (int c = 0; c < 4; ++c) ld16s<0>(f[c], wsr, voffL * 8, WS_FO + c * 8192);
        asm volatile("s_waitcnt vmcnt(0)" : "+v"(f[0]), "+v"(f[1]), "+v"(f[2]), "+v"(f[3]) :: "memory");
#pragma unroll
        for (int c = 0; c < 4; ++c) { const u32x4 v = __builtin_bit_cast(u32x4, f[c]); d |= (v[0] ^ tag[0]) | (v[1] ^ tag[1]); }
        if (all_lanes(d == 0)) break;
        __builtin_amdgcn_s_sleep(32);
      }
      set_flag(tag, wsr, WS_EPOCH);
    }
  } else {
    bf16x8 af[4][4];
    f32x4 dacc[4][4];
    int wa;
    const unsigned soa = WS_HO + (unsigned)(rh * 72 + rg) * 4096u;
    auto apc = [&](auto itc, auto qc) { constexpr int it = decltype(itc)::value, q = decltype(qc)::value; ld16s<1024 * q>(af[it & 3][q], wsr, voffL, soa + it * 16384u); };
    VMW(5); flags_chk(WS_FH + fall);
    sfor<0, 16>([&](auto jc) { constexpr int j = decltype(jc)::value; apc(IC(j >> 2), IC(j & 3)); });
    sfor<0, 18>([&](auto nc) {
      constexpr int n = decltype(nc)::value;
      constexpr auto opsin = [](int s_) { return (s_ <= 13 ? 4 : 0) + (s_ >= 6 && s_ <= 11 ? 4 : 0); };
      constexpr int N = opsin(n - 3) + opsin(n - 2) + opsin(n - 1);
      if constexpr (n == 14) xv = *(const GAS u32x4*)(x + gs + gv());
      if constexpr (n == 10) {
#pragma clang loop unroll(disable)
        for (unsigned spins = 0; spins < (1u << 22); ++spins) { if (rfl(*(volatile LAS unsigned*)(smem + M_CNT + 16 + rg * 4))) break; __builtin_amdgcn_s_sleep(1); }
        }
      asm volatile("s_waitcnt vmcnt(%4)" : "+v"(af[n & 3][0]), "+v"(af[n & 3][1]), "+v"(af[n & 3][2]), "+v"(af[n & 3][3]) : "i"(N) : "memory");
      if constexpr (n == 0) count_flag(lds0 + M_CNT + 4, 7, tag, wsr, WS_FO + r * 2048 + hp * 128);
      s16x4 t[4][2];
      if constexpr (n == 16) { int lz = voffL; asm volatile("" : "+v"(lz)); wa = rg * 4096 + ((lz & 0xf0) << 4) + (((lz >> 8) << 4) ^ (lz & 0xf0)); }
      sfor<0, 16>([&](auto jc) { constexpr int j = decltype(jc)::value, m = j >> 2, q = j & 3;
        if constexpr (n >= 5 && n < 17 && q < 2) t[m][q] = ldtr(bcell(n + 1, m) + q * 2048);
        if constexpr (n >= 6 && n <= 11 && m > 0 && q == 2) { if constexpr (m == 1) bpc(IC(n + 6), IC(0)); bpc(IC(n + 6), IC(m)); }
        SBAR(); MACC(n == 0, dacc[m][q], bq[n % 6][q], af[n & 3][m]); if constexpr (n == 1 && j == 0) SEG(0); else if constexpr (n == 7 && j == 0) SEG(1); else if constexpr (n == 11 && j == 0) SEG(2); else if constexpr (n == 15 && j == 0) SEG(3); else if constexpr (n >= 1 && ((n >= 5 && n < 17) ? q == 0 : (j & 7) == 0)) PAD(); SBAR();
        if constexpr (q == 3 && n <= 13) apc(IC(n + 4), IC(m));
        if constexpr (n == 17 && m >= 2) { asm volatile("" : "+v"(dacc[m - 2][q])); *(LAS f32x4*)(smem + (m - 2) * SLOT + (wa ^ (64 * q))) = dacc[m - 2][q]; SBAR(); } });
      if constexpr (n >= 5 && n < 17) {
#pragma unroll
        for (int q = 0; q < 4; ++q) bq[(n + 1) % 6][q] = cat8(t[q][0], t[q][1]); }
    });
    PAD();
#pragma unroll
    for (int m = 2; m < 4; ++m) { asm volatile("s_nop 3" : "+v"(dacc[m][0]), "+v"(dacc[m][1]), "+v"(dacc[m][2]), "+v"(dacc[m][3]));
#pragma unroll
      for (int q = 0; q < 4; ++q) *(LAS f32x4*)(smem + m * SLOT + (wa ^ (64 * q))) = dacc[m][q];
      SBAR(); }
  }
  {
    PAD();
    unsigned lz = voffL; asm volatile("" : "+v"(lz)); lz >>= 4;
    const int row = 8 * w + (lz >> 3), chunk = lz & 7;
    const u32x4 bv = *(LAS u32x4*)(smem + M_B2 + chunk * 16);
    float xb[8];
#pragma unroll
    for (int c = 0; c < 4; ++c) { xb[2 * c] = bflo(xv[c]) + bflo(bv[c]); xb[2 * c + 1] = bfhi(xv[c]) + bfhi(bv[c]); asm volatile("" : "+v"(xb[2 * c]), "+v"(xb[2 * c + 1])); }
    lptr rp = smem + (row >> 4) * SLOT + (row & 15) * 256 + (((2 * chunk) ^ (row & 15)) << 4), rq = lxor(rp, 16);
    unsigned og = gv();
    asm volatile("" : "+v"(rp), "+v"(rq), "+v"(og));
    PAD(); BAR();
    const f32x4 a0 = (*(LAS f32x4*)rp + *(LAS f32x4*)(rp + 4096)) + (*(LAS f32x4*)(rp + 8192) + *(LAS f32x4*)(rp + 12288));
    const f32x4 a1 = (*(LAS f32x4*)rq + *(LAS f32x4*)(rq + 4096)) + (*(LAS f32x4*)(rq + 8192) + *(LAS f32x4*)(rq + 12288));
    const u32x4 ov = {pk(a0[0] + xb[0], a0[1] + xb[1]), pk(a0[2] + xb[2], a0[3] + xb[3]), pk(a1[0] + xb[4], a1[1] + xb[5]), pk(a1[2] + xb[6], a1[3] + xb[7])};
    __builtin_nontemporal_store(ov, (GAS u32x4*)(out + gs + og));
    asm volatile("7: s_cbranch_execz Lfk_ret\nLfk_end:");
  }
}
}

extern "C" void kernel_launch(void* const* d_in, const int* in_sizes, int n_in, void* d_out, int out_size, void* d_ws, size_t ws_size, hipStream_t stream) {
  fk::fused<<<256, 512, 0, stream>>>((const char*)d_in[0], (const char*)d_in[1], (const char*)d_in[2], (const char*)d_in[4], (const char*)d_in[5],
                                     (const char*)d_in[6], (const char*)d_in[7], (char*)d_out, (char*)d_ws);
}
```
